```python
import torch

def _softmax(x, axis=-1):
    return torch.softmax(x, dim=axis)

def _gelu(x):
    return torch.nn.functional.gelu(x, approximate='tanh')

def _silu(x):
    return torch.nn.functional.silu(x)

def _sigmoid(x):
    return torch.sigmoid(x)

def _layernorm(x, axis=-1, eps=1e-05):
    m = x.mean(dim=axis, keepdim=True)
    v = x.var(dim=axis, keepdim=True, unbiased=False)
    return (x - m) / torch.sqrt(v + eps)

def _scatter_add(tgt, idx, src, axis=0):
    return tgt.clone().index_add_(axis, idx, src)

def _segment_sum(src, seg, n):
    out = torch.zeros((n,) + src.shape[1:], dtype=src.dtype)
    return out.index_add_(0, seg, src)

def _pad(x, pw):
    flat = tuple((v for pair in reversed(pw) for v in pair))
    return torch.nn.functional.pad(x, flat)
kernel_name = 'adaLN_cond__memory_aug_recurrent_mem_tr__glu_gated_bilinear__prompt'

def setup_inputs():

    _w = lambda a: (lambda t: t.to(torch.bfloat16) if t.is_floating_point() else t)(torch.as_tensor(a))

    def _A_setup():
        B, S, Dm = (1, 2048, 512)
        H, Dh = (32, 128)
        M, seg = (8, 32)
        seg = min(seg, S)
        S = S // seg * seg
        return {'x': _w((lambda *s: torch.randn(*s, dtype=torch.bfloat16) * 0.05)(B, S, Dm)), 'mem0': _w((lambda *s: torch.randn(*s, dtype=torch.bfloat16) * 0.8)(B, M, Dm)), 'Wq': _w((lambda *s: torch.randn(*s, dtype=torch.bfloat16) * 1.6)(Dm, H, Dh) * Dm ** (-0.5)), 'Wk': _w((lambda *s: torch.randn(*s, dtype=torch.bfloat16) * 1.6)(Dm, H, Dh) * Dm ** (-0.5)), 'Wv': _w((lambda *s: torch.randn(*s, dtype=torch.bfloat16) * 0.8)(Dm, H, Dh) * Dm ** (-0.5)), 'Wo': _w((lambda *s: torch.randn(*s, dtype=torch.bfloat16) * 0.8)(H, Dh, Dm) * (H * Dh) ** (-0.5)), 'seg_len': seg}
    _A_in = _A_setup()

    def _B_setup():
        B, S, D, F = (1, 2048, 512, 1792)
        return {'x': _w((lambda *s: torch.randn(*s, dtype=torch.bfloat16) * 0.05)(B, S, D)), 'w_gate': _w((lambda *s: torch.randn(*s, dtype=torch.bfloat16) * 0.8)(D, F) * D ** (-0.5)), 'w_up': _w((lambda *s: torch.randn(*s, dtype=torch.bfloat16) * 0.8)(D, F) * D ** (-0.5)), 'w_down': _w((lambda *s: torch.randn(*s, dtype=torch.bfloat16) * 0.8)(F, D) * F ** (-0.5))}
    _B_in = _B_setup()
    return {'A_x': _A_in['x'], 'A_mem0': _A_in['mem0'], 'A_Wq': _A_in['Wq'], 'A_Wk': _A_in['Wk'], 'A_Wv': _A_in['Wv'], 'A_Wo': _A_in['Wo'], 'A_seg_len': _A_in['seg_len'], 'B_x': _B_in['x'], 'B_w_gate': _B_in['w_gate'], 'B_w_up': _B_in['w_up'], 'B_w_down': _B_in['w_down'], 'c': _w((lambda *s: torch.randn(*s, dtype=torch.bfloat16) * 0.1)(1, 512) * 8.0), 'w_mod': _w((lambda *s: torch.randn(*s, dtype=torch.bfloat16) * 0.1)(512, 6 * 512) * 512 ** (-0.5) * 8.0)}

def reference(A_x, A_mem0, A_Wq, A_Wk, A_Wv, A_Wo, A_seg_len, B_x, B_w_gate, B_w_up, B_w_down, c, w_mod):

    def _A_ref(x, mem0, Wq, Wk, Wv, Wo, seg_len):
        B, S, Dm = x.shape
        H, Dh = (Wq.shape[1], Wq.shape[2])
        M = mem0.shape[1]
        scale = Dh ** (-0.5)
        n_seg = S // seg_len
        L = M + seg_len
        idx = torch.arange(L)
        mask = (idx[:, None] < M) | (idx[:, None] >= idx[None, :])
        mem = mem0
        outs = []
        for s in range(n_seg):
            xs = x[:, s * seg_len:(s + 1) * seg_len, :].clone()
            h = torch.concatenate([mem, xs], axis=1)
            q = torch.einsum('btd,dhe->bhte', h, Wq)
            k = torch.einsum('btd,dhe->bhte', h, Wk)
            v = torch.einsum('btd,dhe->bhte', h, Wv)
            scores = torch.einsum('bhqe,bhke->bhqk', q, k) * scale
            scores = torch.where(mask[None, None], scores, -1000000000.0)
            p = _softmax(scores, axis=-1)
            o = torch.einsum('bhqk,bhke->bhqe', p, v)
            o = torch.einsum('bhte,hed->btd', o, Wo)
            mem = o[:, :M, :].clone()
            outs.append(o[:, M:, :])
        return torch.concatenate(outs, axis=1)

    def _B_ref(x, w_gate, w_up, w_down):
        g = torch.einsum('bsd,df->bsf', x, w_gate)
        u = torch.einsum('bsd,df->bsf', x, w_up)
        h = g * u
        return torch.einsum('bsf,fd->bsd', h, w_down)
    x = A_x
    mod = (_silu(c) @ w_mod).reshape(c.shape[0], 6, 1, -1)
    sa, ca, ga, sb, cb, gb = (mod[:, 0].clone(), mod[:, 1].clone(), mod[:, 2].clone(), mod[:, 3].clone(), mod[:, 4].clone(), mod[:, 5].clone())
    h = _layernorm(x, axis=-1) * (1 + ca) + sa
    x = x + ga * _A_ref(h, A_mem0, A_Wq, A_Wk, A_Wv, A_Wo, A_seg_len)
    h = _layernorm(x, axis=-1) * (1 + cb) + sb
    x = x + gb * _B_ref(h, B_w_gate, B_w_up, B_w_down)
    return x
```

```cpp
#include <hip/hip_runtime.h>
#include <hip/hip_bf16.h>
typedef unsigned short u16;
__device__ __forceinline__ float b2f(u16 x) { return __uint_as_float((unsigned)x << 16); }
__device__ __forceinline__ u16 f2b(float f) { unsigned u = __float_as_uint(f); return (u16)((u + 0x7fffu + ((u >> 16) & 1u)) >> 16); }
__device__ __forceinline__ float rb(float f) { return b2f(f2b(f)); }

namespace ref {
constexpr int S = 2048, D = 512, H = 32, E = 128, M = 8, SEG = 32, F = 1792, NSEG = 64;
__global__ void k_mod(const u16* c, const u16* wm, float* mod) {
  int n = blockIdx.x * blockDim.x + threadIdx.x; float a = 0;
  for (int k = 0; k < D; ++k) { float x = b2f(c[k]); float s = rb(x / (1.f + __expf(-x))); a += s * b2f(wm[k * 3072 + n]); }
  mod[n] = rb(a);
}
__global__ void k_ln(const u16* x, const float* sh, const float* sc, u16* out) {
  int r = blockIdx.x, t = threadIdx.x; __shared__ float red[512];
  float v = b2f(x[r * D + t]); red[t] = v; __syncthreads();
  for (int o = 256; o; o >>= 1) { if (t < o) red[t] += red[t + o]; __syncthreads(); }
  float m = red[0] / D; __syncthreads();
  red[t] = (v - m) * (v - m); __syncthreads();
  for (int o = 256; o; o >>= 1) { if (t < o) red[t] += red[t + o]; __syncthreads(); }
  float var = red[0] / D;
  float n = (v - m) * rsqrtf(var + 1e-5f);
  out[r * D + t] = f2b(n * (1.f + sc[t]) + sh[t]);
}
__global__ void k_gemm(const u16* A, const u16* B, u16* C, int Mr, int N, int K) {
  __shared__ float As[16][17], Bs[16][17];
  int tx = threadIdx.x, ty = threadIdx.y; int r = blockIdx.y * 16 + ty, cidx = blockIdx.x * 16 + tx; float a = 0;
  for (int k0 = 0; k0 < K; k0 += 16) {
    As[ty][tx] = r < Mr ? b2f(A[(size_t)r * K + k0 + tx]) : 0.f; Bs[ty][tx] = b2f(B[(size_t)(k0 + ty) * N + cidx]); __syncthreads();
    for (int k = 0; k < 16; ++k) a += As[ty][k] * Bs[k][tx]; __syncthreads();
  }
  if (r < Mr) C[(size_t)r * N + cidx] = f2b(a);
}
__global__ void k_attn(const u16* q, const u16* km, const u16* vm, const u16* kx, const u16* vx, u16* o, int tok, int qseg, int mseg) {
  int h = blockIdx.x, i = blockIdx.y, s = blockIdx.z, t = threadIdx.x; __shared__ float p[40];
  q += (size_t)s * qseg * 4096; o += (size_t)s * qseg * 4096; km += (size_t)s * mseg * 4096; vm += (size_t)s * mseg * 4096; kx += (size_t)s * qseg * 4096; vx += (size_t)s * qseg * 4096;
  if (t < 40) { const u16* kr = t < 8 ? km + t * 4096 : kx + (t - 8) * 4096; float a = 0;
    for (int e = 0; e < E; ++e) a += b2f(q[i * 4096 + h * E + e]) * b2f(kr[h * E + e]);
    a *= 0.08838834764831845f; if (tok && t - 8 > i) a = -1e9f; p[t] = a; }
  __syncthreads();
  float mx = -1e30f; for (int k = 0; k < 40; ++k) mx = fmaxf(mx, p[k]);
  float z = 0; for (int k = 0; k < 40; ++k) z += __expf(p[k] - mx);
  float a = 0; for (int k = 0; k < 40; ++k) { const u16* vr = k < 8 ? vm + k * 4096 : vx + (k - 8) * 4096; a += rb(__expf(p[k] - mx) / z) * b2f(vr[h * E + t]); }
  o[i * 4096 + h * E + t] = f2b(a);
}
__global__ void k_res(const u16* x, const u16* y, const float* g, u16* out) { int i = blockIdx.x * 512 + threadIdx.x; out[i] = f2b(b2f(x[i]) + rb(g[threadIdx.x] * b2f(y[i]))); }
__global__ void k_mul(const u16* a, const u16* b, u16* out) { int i = blockIdx.x * 256 + threadIdx.x; out[i] = f2b(b2f(a[i]) * b2f(b[i])); }
struct Buf { float* mod; u16 *h1, *Q, *K, *V, *mem, *qm, *km, *vm, *om, *O, *att, *x2, *h2, *g, *u, *act, *ff; };
inline Buf carve(void* ws) { char* p = (char*)ws; Buf b; auto a = [&](size_t n) { char* r = p; p += (n + 255) & ~(size_t)255; return r; };
  b.mod = (float*)a(3072 * 4); b.h1 = (u16*)a(S * D * 2); b.Q = (u16*)a((size_t)S * 4096 * 2); b.K = (u16*)a((size_t)S * 4096 * 2); b.V = (u16*)a((size_t)S * 4096 * 2);
  b.mem = (u16*)a(65 * 8 * D * 2); b.qm = (u16*)a(8 * 4096 * 2); b.km = (u16*)a(64 * 8 * 4096 * 2); b.vm = (u16*)a(64 * 8 * 4096 * 2); b.om = (u16*)a(8 * 4096 * 2);
  b.O = (u16*)a((size_t)S * 4096 * 2); b.att = (u16*)a(S * D * 2); b.x2 = (u16*)a(S * D * 2); b.h2 = (u16*)a(S * D * 2); b.g = (u16*)a(S * F * 2); b.u = (u16*)a(S * F * 2); b.act = (u16*)a(S * F * 2); b.ff = (u16*)a(S * D * 2); return b; }
inline void run(void* const* d_in, void* d_out, void* ws, hipStream_t st) {
  const u16 *x = (const u16*)d_in[0], *mem0 = (const u16*)d_in[1], *Wq = (const u16*)d_in[2], *Wk = (const u16*)d_in[3], *Wv = (const u16*)d_in[4], *Wo = (const u16*)d_in[5];
  const u16 *wg = (const u16*)d_in[8], *wu = (const u16*)d_in[9], *wd = (const u16*)d_in[10], *c = (const u16*)d_in[11], *wm = (const u16*)d_in[12];
  Buf b = carve(ws); dim3 t16(16, 16);
  k_mod<<<12, 256, 0, st>>>(c, wm, b.mod);
  k_ln<<<S, 512, 0, st>>>(x, b.mod, b.mod + 512, b.h1);
  k_gemm<<<dim3(256, 128), t16, 0, st>>>(b.h1, Wq, b.Q, S, 4096, 512);
  k_gemm<<<dim3(256, 128), t16, 0, st>>>(b.h1, Wk, b.K, S, 4096, 512);
  k_gemm<<<dim3(256, 128), t16, 0, st>>>(b.h1, Wv, b.V, S, 4096, 512);
  hipMemcpyAsync(b.mem, mem0, 8 * D * 2, hipMemcpyDeviceToDevice, st);
  for (int s = 0; s < NSEG; ++s) {
    u16* m = b.mem + s * 8 * D; u16* km = b.km + s * 8 * 4096; u16* vm = b.vm + s * 8 * 4096;
    k_gemm<<<dim3(256, 1), t16, 0, st>>>(m, Wq, b.qm, 8, 4096, 512);
    k_gemm<<<dim3(256, 1), t16, 0, st>>>(m, Wk, km, 8, 4096, 512);
    k_gemm<<<dim3(256, 1), t16, 0, st>>>(m, Wv, vm, 8, 4096, 512);
    k_attn<<<dim3(32, 8, 1), 128, 0, st>>>(b.qm, km, vm, b.K + (size_t)s * 32 * 4096, b.V + (size_t)s * 32 * 4096, b.om, 0, 0, 0);
    k_gemm<<<dim3(32, 1), t16, 0, st>>>(b.om, Wo, m + 8 * D, 8, 512, 4096);
  }
  k_attn<<<dim3(32, 32, 64), 128, 0, st>>>(b.Q, b.km, b.vm, b.K, b.V, b.O, 1, 32, 8);
  k_gemm<<<dim3(32, 128), t16, 0, st>>>(b.O, Wo, b.att, S, 512, 4096);
  k_res<<<S, 512, 0, st>>>(x, b.att, b.mod + 1024, b.x2);
  k_ln<<<S, 512, 0, st>>>(b.x2, b.mod + 1536, b.mod + 2048, b.h2);
  k_gemm<<<dim3(112, 128), t16, 0, st>>>(b.h2, wg, b.g, S, F, 512);
  k_gemm<<<dim3(112, 128), t16, 0, st>>>(b.h2, wu, b.u, S, F, 512);
  k_mul<<<S * F / 256, 256, 0, st>>>(b.g, b.u, b.act);
  k_gemm<<<dim3(32, 128), t16, 0, st>>>(b.act, wd, b.ff, S, 512, F);
  k_res<<<S, 512, 0, st>>>(b.x2, b.ff, b.mod + 2560, (u16*)d_out);
}
}
extern "C" void kernel_launch(void* const* d_in, const int* in_sizes, int n_in, void* d_out, int out_size, void* d_ws, size_t ws_size, hipStream_t stream) {
  ref::run(d_in, d_out, d_ws, stream);
}
```

```cpp
#include <hip/hip_runtime.h>
#include <hip/hip_bf16.h>

typedef unsigned short u16;
typedef unsigned v16u __attribute__((ext_vector_type(16)));
typedef unsigned v8u __attribute__((ext_vector_type(8)));
typedef unsigned v4u __attribute__((ext_vector_type(4)));
typedef unsigned v2u __attribute__((ext_vector_type(2)));
typedef float v4f __attribute__((ext_vector_type(4)));
#define GAS __attribute__((address_space(1)))
#define LAS __attribute__((address_space(3)))
#define SENT 0x7F817F81u
#define DEV __device__ __forceinline__

#define CFG_CODEKB 56
#define CFG_SPOLL 0
#define CFG_QPOLL 2
#define CFG_QDEL 0
#define CFG_GDEL 2
#define CFG_QGATE 2
#define CFG_TW 0
#define CFG_TRK 0
#define CFG_OWT 1
#define CFG_NWT 1
#define CFG_STAG 0
#define CFG_NS 1
#define CFG_PWN 32
#define CFG_PWF 32
#define CFG_PW 0
#define CFG_MW 7
#define CFG_WDEPTH 3
#define CFG_PDELAY 0
#define CFG_PPOLL 4
#define CFG_TPOLL 3
#define CFG_PF 1
#define CFG_PACE 1
#define CFG_QLEAD 5
#define CFG_PLEAD 3
#define CFG_D1 320
#define CFG_D2 1190

constexpr unsigned REG = 2u << 20;
constexpr int RG_COMM = 0;
constexpr int RG_KV = 1;
constexpr int RG_Q = 33;
constexpr int RG_KM = 49;
constexpr int RG_H1 = 65;
constexpr int RG_O = 67;
constexpr int RG_PW = 83;
constexpr int RG_ACT = 91;
constexpr int RG_H2 = 99;
constexpr int RG_X2 = 101;
constexpr int RG_FL = 103;
constexpr int NPROBE = 104;
constexpr size_t WS_COMM = 0;
constexpr size_t WS_CTL = (size_t)104 * REG;
constexpr int COMM_PROGA = 49 * 16384;
constexpr int CTL_PROGP = 8192;
constexpr int CTL_TICKET = 32768;
constexpr int CTL_PROGL = 33792;
constexpr int CTL_PAR = 50176;
constexpr int CTL_MOD = 53248;
constexpr size_t WS_N1 = (size_t)105 * REG;
constexpr size_t WS_DBG = (size_t)106 * REG;
DEV unsigned hm(unsigned o) { return o + (o & ~8191u); }

struct Params {
  const u16 *x, *mem0, *Wq, *Wk, *Wv, *Wo, *wg, *wu, *wd, *c, *wm;
  u16* out;
  char* ws;
  int d1, d2;
};
static int g_d1 = CFG_D1, g_d2 = CFG_D2;

DEV float b2f(unsigned x) { return __uint_as_float(x << 16); }
DEV float blo(unsigned x) { return __uint_as_float(x << 16); }
DEV float bhi(unsigned x) { return __uint_as_float(x & 0xffff0000u); }
DEV unsigned pk2(float a, float b) {
  typedef __bf16 bf2 __attribute__((ext_vector_type(2)));
  bf2 r = {(__bf16)a, (__bf16)b};
  unsigned u = __builtin_bit_cast(unsigned, r);
  asm("" : "+v"(u));
  return u;
}
DEV float rb(float a) { return blo(pk2(a, a)); }
DEV unsigned xcc_id() { return (unsigned)__builtin_amdgcn_s_getreg(20 | (3 << 11)) & 0xfu; }
template <int CTRL, int RM>
DEV float dpp_add(float v) { return v + __builtin_bit_cast(float, __builtin_amdgcn_update_dpp(0, __builtin_bit_cast(int, v), CTRL, RM, 0xf, false)); }
DEV float wave_sum(float v) {
  v = dpp_add<0x111, 0xf>(v); v = dpp_add<0x112, 0xf>(v); v = dpp_add<0x114, 0xf>(v); v = dpp_add<0x118, 0xf>(v);
  v = dpp_add<0x142, 0xa>(v); v = dpp_add<0x143, 0xc>(v);
  return __builtin_bit_cast(float, __builtin_amdgcn_readlane(__builtin_bit_cast(int, v), 63));
}
DEV unsigned vote(unsigned r) {
  const unsigned b0 = (unsigned)__ballot(r & 1) & 255u, b1 = (unsigned)__ballot(r & 2) & 255u, lo = (unsigned)__ballot(!(r & 4)) & 255u;
  return (__popc(b0) > 4 || (__popc(b0) == 4 && (b0 & 1)) ? 1u : 0u) | (__popc(b1) + 2 * __popc(b1 & lo) > 4 + __popc(lo) ? 2u : 0u);
}
DEV unsigned comm_vote(const char* ws) { return vote(((const unsigned*)(ws + WS_CTL + CTL_PAR))[112 + (threadIdx.x & 7)]); }
DEV v4u ln_norm(const float (&v)[8]) {
  float s = 0;
#pragma unroll
  for (int i = 0; i < 8; ++i) s += v[i];
  const float mean = wave_sum(s) * (1.f / 512.f);
  float q = 0;
#pragma unroll
  for (int i = 0; i < 8; ++i) q += (v[i] - mean) * (v[i] - mean);
  const float var = rb(wave_sum(q) * (1.f / 512.f));
  const float m = rb(mean), rs = __builtin_amdgcn_rcpf(rb(__builtin_amdgcn_sqrtf(rb(var + 1e-5f))));
  v4u n;
#pragma unroll
  for (int i = 0; i < 4; ++i) { const unsigned d = pk2(v[2 * i] - m, v[2 * i + 1] - m); n[i] = pk2(blo(d) * rs, bhi(d) * rs); }
  return n;
}
DEV v4u ln_mod(v4u n, v4u t, v4u sh) {
  v4u y;
#pragma unroll
  for (int i = 0; i < 4; ++i) { const unsigned u = pk2(blo(n[i]) * blo(t[i]), bhi(n[i]) * bhi(t[i])); y[i] = pk2(blo(u) + blo(sh[i]), bhi(u) + bhi(sh[i])); }
  return y;
}

DEV void warm(const char* a, unsigned step) {
  v4u t[16];
#pragma unroll
  for (int i = 0; i < 16; ++i) t[i] = *(const v4u*)(a + (size_t)i * step);
#pragma unroll
  for (int i = 0; i < 16; ++i) asm volatile("" ::"v"(t[i]));
}
constexpr int PB_MOD = 0, PB_LN = 192, PB_X = 320 - 48, PREP_W0 = 320 + 200;
__global__ void __launch_bounds__(256) k_prep(Params p) {
  __shared__ __attribute__((aligned(16))) float red[16 * 264];
  const int tid = threadIdx.x, l = tid & 63, w = tid >> 6, b = (int)blockIdx.x - PB_X;
  char* ctl = p.ws + WS_CTL;
  if (CFG_TW && blockIdx.x < 16) {
    const char* a;
    if (blockIdx.x < 8) {
      const int k = tid - 112, m = k >> 2;
      const char* in = (const char*)(m == 0 ? p.Wq : m == 1 ? p.Wk : m == 2 ? p.Wv : m == 3 ? p.Wo : m == 4 ? p.wg : m == 5 ? p.wu : p.wd);
      a = tid < 106 ? p.ws + (size_t)tid * REG + 126 * 16384 + 4096 : (tid >= 112 && tid < 140 ? in + (k & 3) * (m < 4 ? 1398080 : 611648) : (const char*)p.c);
    } else {
      asm volatile("s_getpc_b64 %0" : "=s"(a));
      a = (const char*)(((size_t)a & ~(size_t)255) + 4096 + tid * 216);
    }
    const unsigned t0 = *(const GAS unsigned*)a, t1 = *(const GAS unsigned*)(a + (blockIdx.x < 8 ? 0 : 108));
    asm volatile("" ::"v"(t0), "v"(t1));
  }
  if (blockIdx.x >= PREP_W0) {
    const unsigned x = xcc_id() & 7, k = ((blockIdx.x - PREP_W0) >> 3) & 31;
    if (x < 4) { if (k < CFG_PWN) warm((const char*)(k < 16 ? p.Wq : p.Wk) + (size_t)((k & 15) * 32 + (tid >> 7)) * 8192 + x * 2048 + (tid & 127) * 16, 16384); }
    else if (k < CFG_PWF) warm((const char*)(x < 6 ? p.Wk : p.Wv) + (size_t)(k * 16) * 8192 + (x & 1) * 4096 + tid * 16, 8192);
  } else if (blockIdx.x < PB_LN) {
    const int bb = blockIdx.x, cg = ((bb >> 5) * 8 + (bb & 7)) * 4 + ((bb >> 3) & 3), kq = tid >> 1;
    v4u wv[4];
#pragma unroll
    for (int i = 0; i < 4; ++i) wv[i] = *(const v4u*)(p.wm + (size_t)(kq * 4 + i) * 3072 + cg * 16 + (tid & 1) * 8);
    const v2u cw = *(const v2u*)(p.c + kq * 4);
    float a[8] = {0, 0, 0, 0, 0, 0, 0, 0};
#pragma unroll
    for (int i = 0; i < 4; ++i) {
      const float x = (i & 1) ? bhi(cw[i >> 1]) : blo(cw[i >> 1]);
      const float s = rb(x / (1.f + __expf(-x)));
#pragma unroll
      for (int j = 0; j < 4; ++j) { a[2 * j] += s * blo(wv[i][j]); a[2 * j + 1] += s * bhi(wv[i][j]); }
    }
#pragma unroll
    for (int j = 0; j < 8; ++j) red[((tid & 1) * 8 + j) * 132 + kq] = a[j];
    __syncthreads();
    const int col = tid >> 4;
    float s = 0;
#pragma unroll
    for (int i = 0; i < 8; ++i) s += red[col * 132 + (tid & 15) + 16 * i];
    s = dpp_add<0x111, 0xf>(s); s = dpp_add<0x112, 0xf>(s); s = dpp_add<0x114, 0xf>(s); s = dpp_add<0x118, 0xf>(s);
    if ((tid & 15) == 15) {
      const int n = cg * 16 + col, part = n >> 9;
      s = rb(s);
      ((u16*)(ctl + CTL_MOD))[n] = (u16)pk2((part == 1 || part == 4) ? 1.f + s : s, 0.f);
    }
  } else if (b < 48) {
    const int hs = blockIdx.x - PB_LN;
    v4u xv[4];
#pragma unroll
    for (int r = 0; r < 4; ++r) xv[r] = *(const v4u*)(p.x + (hs * 16 + w * 4 + r) * 512 + l * 8);
#pragma unroll
    for (int r = 0; r < 4; ++r) {
      float v[8];
#pragma unroll
      for (int j = 0; j < 4; ++j) { v[2 * j] = blo(xv[r][j]); v[2 * j + 1] = bhi(xv[r][j]); }
      *(v4u*)(red + (w * 4 + r) * 264 + l * 4) = ln_norm(v);
    }
    __syncthreads();
#pragma unroll
    for (int k = 0; k < 4; ++k) {
      char* d = p.ws + WS_N1 + hs * 16384 + (w * 4 + k) * 1024 + l * 16;
      const v4u v = *(const v4u*)(red + (l & 15) * 264 + (w * 4 + k) * 16 + (l >> 4) * 4);
      if (CFG_NWT) asm volatile("global_store_dwordx4 %0, %1, off sc1\n\ts_nop 1" ::"v"(d), "v"(v) : "memory"); else *(v4u*)d = v;
    }
  } else if (b == 48) {
    for (int i = tid; i < CTL_PAR / 4; i += 256) ((unsigned*)ctl)[i] = 0;
  } else if (b >= 131 && b < 136) {
    if (b < 135) for (int i = tid; i < 1024; i += 256) *(v4u*)(p.ws + (size_t)RG_FL * REG + (b - 131) * 16384 + i * 16) = (v4u){0u, 0u, 0u, 0u};
  } else if (b >= 136) {
    const bool cm = b < 144;
    const int rg = cm ? RG_COMM : b - 143;
    unsigned lat[4] = {1u << 20, 1u << 20, 1u << 20, 1u << 20};
    if (l == 0 && rg < NPROBE) {
#pragma unroll
      for (int a = 0; a < 6; ++a) {
        if (a < 4 || cm) {
          const int k = a < 4 ? (a ^ (w >> 1)) & 1 : (lat[1] < lat[0] ? 3 : 2);
          unsigned* q = (unsigned*)(p.ws + (size_t)rg * REG + (120 + w * 2) * 16384 + (b & 7) * 256 + (w & 1) * 4096 + k * 8192);
          const unsigned long long t0 = __builtin_amdgcn_s_memtime();
          asm volatile("global_store_dword %0, %1, off sc1\n\ts_waitcnt vmcnt(0)" ::"v"(q), "v"(0u) : "memory");
          const unsigned d = (unsigned)(__builtin_amdgcn_s_memtime() - t0);
          if (k == 0) lat[0] = min(d, lat[0]); else if (k == 1) lat[1] = min(d, lat[1]); else if (k == 2) lat[2] = min(d, lat[2]); else lat[3] = min(d, lat[3]);
        }
      }
    }
    unsigned* ru = (unsigned*)red;
    if (l == 0) { ru[w * 4] = lat[0]; ru[w * 4 + 1] = lat[1]; ru[w * 4 + 2] = lat[2]; ru[w * 4 + 3] = lat[3]; }
    __syncthreads();
    if (tid == 0 && rg < NPROBE) {
      const unsigned xc = xcc_id();
      const unsigned own = min(min(ru[1], ru[5]), min(ru[9], ru[13])) < min(min(ru[0], ru[4]), min(ru[8], ru[12])) ? 1u : 0u;
      const unsigned low = own ^ ((xc >> 2) & 1u);
      if (cm) {
        const unsigned odd = min(ru[2 + own], ru[10 + own]) + min(ru[6 + own], ru[14 + own]) < min(ru[own], ru[8 + own]) + min(ru[4 + own], ru[12 + own]) ? 1u : 0u;
        ((unsigned*)(ctl + CTL_PAR))[112 + b - 136] = low | ((odd ^ (xc & 1u)) << 1) | (xc & 4u);
      } else ((unsigned*)(ctl + CTL_PAR))[rg] = ((rg >= RG_KM && rg < RG_H1) ? low : low ^ 1u) * 8192u;
    }
  } else {
    const int blk = b - 49;
#pragma unroll
    for (int r = 0; r < 4; ++r) {
      const int o16 = r * 256 + tid, o = (o16 & 511) * 16;
      v4u v = {SENT, SENT, SENT, SENT};
      if (blk == 80) v = (v4u){~0u, ~0u, ~0u, ~0u};
      if (blk == 81) v = (v4u){0u, 0u, 0u, 0u};
      if (blk >= 64 && blk < 68) {
        const int t = o >> 10, line = (o >> 7) & 7, pos = (o >> 4) & 7;
        const int g = (line & 1) * 2 + (pos >> 2), row = (line >> 1) * 2 + (pos & 1), pp = (pos >> 1) & 1;
        const u16* m = p.mem0 + row * 512 + 64 * t + 4 * g + 2 * pp;
#pragma unroll
        for (int j = 0; j < 4; ++j) v[j] = m[16 * ((2 * j) & 3) + (j >> 1)] | ((unsigned)m[16 * ((2 * j + 1) & 3) + (j >> 1)] << 16);
      }
      char* d = p.ws + WS_COMM + (blk < 32 ? blk : (blk < 64 ? blk + 32 : blk - 32)) * 16384 + o16 * 16;
      asm volatile("global_store_dwordx4 %0, %1, off sc1\n\ts_nop 1" ::"v"(d), "v"(v) : "memory");
    }
  }
}

constexpr int MEMST = 0, XB = 8192, RS = 288, QOFF = 24576, KOFF = QOFF + 8 * RS, OOFF = KOFF + 8 * RS, WL = 32768;
#define V10(a) "v" #a "0","v" #a "1","v" #a "2","v" #a "3","v" #a "4","v" #a "5","v" #a "6","v" #a "7","v" #a "8","v" #a "9"
#define A10(a) "a" #a "0","a" #a "1","a" #a "2","a" #a "3","a" #a "4","a" #a "5","a" #a "6","a" #a "7","a" #a "8","a" #a "9"
#define S10(a) "s" #a "0","s" #a "1","s" #a "2","s" #a "3","s" #a "4","s" #a "5","s" #a "6","s" #a "7","s" #a "8","s" #a "9"
DEV unsigned lo32(const void* p) { return (unsigned)(size_t)p; }
DEV unsigned hi32(const void* p) { return (unsigned)((size_t)p >> 32); }
DEV void a_role(const Params& p, int h, int j, unsigned xi) {
  const int tid = threadIdx.x, w = tid >> 6, l = tid & 63, g = l >> 4, c = l & 15;
  const int r = 32 * j + h;
  char* ctl = p.ws + WS_CTL;
  const unsigned* par = (const unsigned*)(ctl + CTL_PAR);
  const unsigned cv = comm_vote(p.ws), e1 = (cv >> 1) ^ (CFG_NS == 2);
  auto rpos = [&](int rr) { const int u = (rr >> 4) * 8 + (rr & 7); return (unsigned)(((u >> 1) * 2 + (((rr >> 3) & 1) ^ e1 ^ 1)) * 16384 + (u & 1) * 4096); };
  xi ^= e1;
  v16u va; v8u vb; unsigned vkp;
  va[0] = (c & 2) ? 0xEEEEEEEEu : 0x44444444u;
  va[1] = l * 16;
  va[2] = MEMST + 2 * w * 1024 + l * 16;
  va[3] = MEMST + 2 * (w ^ 1) * 1024 + l * 16; va[4] = MEMST + 2 * (w ^ 2) * 1024 + l * 16; va[5] = MEMST + 2 * (w ^ 3) * 1024 + l * 16;
  va[6] = QOFF + 2 * g * RS + 64 * w + 4 * c; va[7] = va[6] + (KOFF - QOFF);
  va[8] = QOFF + (c & 7) * RS + 16 * g; va[9] = KOFF + (2 * (c >> 2) + (c & 1)) * RS + 16 * g;
  va[10] = OOFF + (w >> 1) * 1024 + (16 * (2 * (w & 1) + (g >> 1)) + (g & 1) * 8 + (c & 7)) * 16;
  va[11] = OOFF + (16 * g + ((c >> 1) & 1) * 8 + 2 * (c >> 2) + (c & 1)) * 16;
  va[12] = WL + w * 24576 + l * 16;
  va[13] = 2 * w * 1024 + (2 * (c >> 2) + (g >> 1)) * 128 + (4 * (g & 1) + (c & 3)) * 16;
  { const int rr = 32 * j + 8 * w + (l >> 3); va[14] = rpos(rr) + h * 128 + (l & 7) * 16; }
  { const int n = c & 7; va[15] = ((g >> 1) * 2 + (c >> 3)) * 16384 + r * 128 + (2 * (n >> 1) + (g & 1)) * 16 + (n & 1) * 8; }
  const int q = (l >> 2) & 3, pp = l & 3; const unsigned swz = ((pp ^ (2 * (g & 1))) * 16);
  vb[0] = (16 * (q & 1) + 2 * (q >> 1) + 4 * (g >> 1) + (g & 1)) * 8192 + h * 256 + w * 64 + swz;
  vb[1] = (h * 128 + 16 * (g & 1) + 8 * (g >> 1) + 4 * (q >> 1) + (q & 1)) * 1024 + j * 512 + w * 128 + swz;
  vb[2] = w * 40960 + (g * 4 + q) * 64 + ((0 ^ (g & 1)) * 32) + pp * 8;
  vb[3] = w * 40960 + (g * 4 + q) * 64 + ((1 ^ (g & 1)) * 32) + pp * 8;
  { const int m = l & 7; const unsigned one = 0x3f80u << ((m & 1) * 16);
#pragma unroll
    for (int i = 0; i < 4; ++i) vb[4 + i] = (m >> 1) == i ? one : 0u; }
  vkp = KOFF + (2 * w + (l >> 5)) * RS + (l & 31) * 8;
  v16u sa; v8u sb; v2u sc;
  sa[0] = lo32(p.Wq); sa[1] = hi32(p.Wq); sa[2] = lo32(p.Wk); sa[3] = hi32(p.Wk); sa[4] = lo32(p.Wv); sa[5] = hi32(p.Wv); sa[6] = lo32(p.Wo); sa[7] = hi32(p.Wo);
  const char* cm = p.ws + WS_COMM + (cv & 1) * 8192; sa[8] = lo32(cm); sa[9] = hi32(cm);
  const char* kv = p.ws + (size_t)(RG_KV + h) * REG + par[RG_KV + h]; sa[10] = lo32(kv); sa[11] = hi32(kv);
  const char* km = p.ws + (size_t)(RG_KM + (h >> 1)) * REG + par[RG_KM + (h >> 1)] + (h & 1) * 4096 + j * 2048 + w * 512; sa[12] = lo32(km); sa[13] = hi32(km);
  const char* pa = cm + COMM_PROGA + r * 128; sa[14] = lo32(pa); sa[15] = hi32(pa);
  const char* pn = ctl + CTL_PROGP + (16 + (h >> 1)) * 512; sb[0] = lo32(pn); sb[1] = hi32(pn);
  sb[2] = w; sb[3] = j; sb[4] = r; sb[5] = p.d1; sb[6] = p.d2; sb[7] = __float_as_uint(0.08838834764831845f * 1.4426950408889634f);

  const char* db = p.ws + WS_DBG + (r * 4 + w) * 64;
 sc[0] = lo32(db); sc[1] = hi32(db);
  unsigned ro = rpos(r);
  asm volatile(
"s_nop 1\n"
"v_readfirstlane_b32 s40, v200\n"
"v_readfirstlane_b32 s41, v201\n"
"v_readfirstlane_b32 s42, v202\n"
"v_readfirstlane_b32 s43, v203\n"
"v_readfirstlane_b32 s44, v204\n"
"v_readfirstlane_b32 s45, v205\n"
"v_readfirstlane_b32 s46, v206\n"
"v_readfirstlane_b32 s47, v207\n"
"v_readfirstlane_b32 s48, v208\n"
"v_readfirstlane_b32 s49, v209\n"
"v_readfirstlane_b32 s50, v210\n"
"v_readfirstlane_b32 s51, v211\n"
"v_readfirstlane_b32 s52, v212\n"
"v_readfirstlane_b32 s53, v213\n"
"v_readfirstlane_b32 s54, v214\n"
"v_readfirstlane_b32 s55, v215\n"
"v_readfirstlane_b32 s56, v216\n"
"v_readfirstlane_b32 s57, v217\n"
"v_readfirstlane_b32 s58, v218\n"
"v_readfirstlane_b32 s59, v219\n"
"v_readfirstlane_b32 s60, v220\n"
"v_readfirstlane_b32 s61, v221\n"
"v_readfirstlane_b32 s62, v222\n"
"v_readfirstlane_b32 s63, v223\n"
"v_readfirstlane_b32 s64, v252\n"
"v_readfirstlane_b32 s65, v253\n"
"v_readfirstlane_b32 s71, v254\n"
"v_readfirstlane_b32 s91, v255\n"
"s_nop 4\n"
"s_mul_i32 s90, s58, 40960\n"
"s_xor_b32 s74, s58, 0\n"
"s_lshl_b32 s74, s74, 20\n"
"s_add_u32 s75, s74, 0\n"
"v_add_u32 v252, s75, v240\n"
"s_add_u32 m0, s90, 0\n"
"s_nop 0\n"
"global_load_lds_dwordx4 v252, s[42:43]\n"
"s_add_u32 s75, s74, 262144\n"
"v_add_u32 v252, s75, v240\n"
"s_add_u32 m0, s90, 1024\n"
"s_nop 0\n"
"global_load_lds_dwordx4 v252, s[42:43]\n"
"s_add_u32 s75, s74, 65536\n"
"v_add_u32 v252, s75, v240\n"
"s_add_u32 m0, s90, 2048\n"
"s_nop 0\n"
"global_load_lds_dwordx4 v252, s[42:43]\n"
"s_add_u32 s75, s74, 327680\n"
"v_add_u32 v252, s75, v240\n"
"s_add_u32 m0, s90, 3072\n"
"s_nop 0\n"
"global_load_lds_dwordx4 v252, s[42:43]\n"
"s_xor_b32 s74, s58, 0\n"
"s_lshl_b32 s74, s74, 20\n"
"s_add_u32 s75, s74, 0\n"
"v_add_u32 v252, s75, v240\n"
"s_add_u32 m0, s90, 4096\n"
"s_nop 0\n"
"global_load_lds_dwordx4 v252, s[44:45]\n"
"s_add_u32 s75, s74, 262144\n"
"v_add_u32 v252, s75, v240\n"
"s_add_u32 m0, s90, 5120\n"
"s_nop 0\n"
"global_load_lds_dwordx4 v252, s[44:45]\n"
"s_add_u32 s75, s74, 65536\n"
"v_add_u32 v252, s75, v240\n"
"s_add_u32 m0, s90, 6144\n"
"s_nop 0\n"
"global_load_lds_dwordx4 v252, s[44:45]\n"
"s_add_u32 s75, s74, 327680\n"
"v_add_u32 v252, s75, v240\n"
"s_add_u32 m0, s90, 7168\n"
"s_nop 0\n"
"global_load_lds_dwordx4 v252, s[44:45]\n"
"s_xor_b32 s74, s58, 0\n"
"s_lshl_b32 s74, s74, 20\n"
"s_add_u32 s75, s74, 524288\n"
"v_add_u32 v252, s75, v240\n"
"s_add_u32 m0, s90, 8192\n"
"s_nop 0\n"
"global_load_lds_dwordx4 v252, s[42:43]\n"
"s_add_u32 s75, s74, 786432\n"
"v_add_u32 v252, s75, v240\n"
"s_add_u32 m0, s90, 9216\n"
"s_nop 0\n"
"global_load_lds_dwordx4 v252, s[42:43]\n"
"s_add_u32 s75, s74, 589824\n"
"v_add_u32 v252, s75, v240\n"
"s_add_u32 m0, s90, 10240\n"
"s_nop 0\n"
"global_load_lds_dwordx4 v252, s[42:43]\n"
"s_add_u32 s75, s74, 851968\n"
"v_add_u32 v252, s75, v240\n"
"s_add_u32 m0, s90, 11264\n"
"s_nop 0\n"
"global_load_lds_dwordx4 v252, s[42:43]\n"
"s_xor_b32 s74, s58, 0\n"
"s_lshl_b32 s74, s74, 20\n"
"s_add_u32 s75, s74, 524288\n"
"v_add_u32 v252, s75, v240\n"
"s_add_u32 m0, s90, 12288\n"
"s_nop 0\n"
"global_load_lds_dwordx4 v252, s[44:45]\n"
"s_add_u32 s75, s74, 786432\n"
"v_add_u32 v252, s75, v240\n"
"s_add_u32 m0, s90, 13312\n"
"s_nop 0\n"
"global_load_lds_dwordx4 v252, s[44:45]\n"
"s_add_u32 s75, s74, 589824\n"
"v_add_u32 v252, s75, v240\n"
"s_add_u32 m0, s90, 14336\n"
"s_nop 0\n"
"global_load_lds_dwordx4 v252, s[44:45]\n"
"s_add_u32 s75, s74, 851968\n"
"v_add_u32 v252, s75, v240\n"
"s_add_u32 m0, s90, 15360\n"
"s_nop 0\n"
"global_load_lds_dwordx4 v252, s[44:45]\n"
"s_xor_b32 s74, s58, 1\n"
"s_lshl_b32 s74, s74, 20\n"
"s_add_u32 s75, s74, 0\n"
"v_add_u32 v252, s75, v240\n"
"s_add_u32 m0, s90, 16384\n"
"s_nop 0\n"
"global_load_lds_dwordx4 v252, s[42:43]\n"
"s_add_u32 s75, s74, 262144\n"
"v_add_u32 v252, s75, v240\n"
"s_add_u32 m0, s90, 17408\n"
"s_nop 0\n"
"global_load_lds_dwordx4 v252, s[42:43]\n"
"s_add_u32 s75, s74, 65536\n"
"v_add_u32 v252, s75, v240\n"
"s_add_u32 m0, s90, 18432\n"
"s_nop 0\n"
"global_load_lds_dwordx4 v252, s[42:43]\n"
"s_add_u32 s75, s74, 327680\n"
"v_add_u32 v252, s75, v240\n"
"s_add_u32 m0, s90, 19456\n"
"s_nop 0\n"
"global_load_lds_dwordx4 v252, s[42:43]\n"
"s_xor_b32 s74, s58, 1\n"
"s_lshl_b32 s74, s74, 20\n"
"s_add_u32 s75, s74, 0\n"
"v_add_u32 v252, s75, v240\n"
"s_add_u32 m0, s90, 20480\n"
"s_nop 0\n"
"global_load_lds_dwordx4 v252, s[44:45]\n"
"s_add_u32 s75, s74, 262144\n"
"v_add_u32 v252, s75, v240\n"
"s_add_u32 m0, s90, 21504\n"
"s_nop 0\n"
"global_load_lds_dwordx4 v252, s[44:45]\n"
"s_add_u32 s75, s74, 65536\n"
"v_add_u32 v252, s75, v240\n"
"s_add_u32 m0, s90, 22528\n"
"s_nop 0\n"
"global_load_lds_dwordx4 v252, s[44:45]\n"
"s_add_u32 s75, s74, 327680\n"
"v_add_u32 v252, s75, v240\n"
"s_add_u32 m0, s90, 23552\n"
"s_nop 0\n"
"global_load_lds_dwordx4 v252, s[44:45]\n"
"s_xor_b32 s74, s58, 1\n"
"s_lshl_b32 s74, s74, 20\n"
"s_add_u32 s75, s74, 524288\n"
"v_add_u32 v252, s75, v240\n"
"s_add_u32 m0, s90, 24576\n"
"s_nop 0\n"
"global_load_lds_dwordx4 v252, s[42:43]\n"
"s_add_u32 s75, s74, 786432\n"
"v_add_u32 v252, s75, v240\n"
"s_add_u32 m0, s90, 25600\n"
"s_nop 0\n"
"global_load_lds_dwordx4 v252, s[42:43]\n"
"s_add_u32 s75, s74, 589824\n"
"v_add_u32 v252, s75, v240\n"
"s_add_u32 m0, s90, 26624\n"
"s_nop 0\n"
"global_load_lds_dwordx4 v252, s[42:43]\n"
"s_add_u32 s75, s74, 851968\n"
"v_add_u32 v252, s75, v240\n"
"s_add_u32 m0, s90, 27648\n"
"s_nop 0\n"
"global_load_lds_dwordx4 v252, s[42:43]\n"
"s_xor_b32 s74, s58, 1\n"
"s_lshl_b32 s74, s74, 20\n"
"s_add_u32 s75, s74, 524288\n"
"v_add_u32 v252, s75, v240\n"
"s_add_u32 m0, s90, 28672\n"
"s_nop 0\n"
"global_load_lds_dwordx4 v252, s[44:45]\n"
"s_add_u32 s75, s74, 786432\n"
"v_add_u32 v252, s75, v240\n"
"s_add_u32 m0, s90, 29696\n"
"s_nop 0\n"
"global_load_lds_dwordx4 v252, s[44:45]\n"
"s_add_u32 s75, s74, 589824\n"
"v_add_u32 v252, s75, v240\n"
"s_add_u32 m0, s90, 30720\n"
"s_nop 0\n"
"global_load_lds_dwordx4 v252, s[44:45]\n"
"s_add_u32 s75, s74, 851968\n"
"v_add_u32 v252, s75, v240\n"
"s_add_u32 m0, s90, 31744\n"
"s_nop 0\n"
"global_load_lds_dwordx4 v252, s[44:45]\n"
"s_xor_b32 s74, s58, 2\n"
"s_lshl_b32 s74, s74, 20\n"
"s_add_u32 s75, s74, 0\n"
"v_add_u32 v252, s75, v240\n"
"s_add_u32 m0, s90, 32768\n"
"s_nop 0\n"
"global_load_lds_dwordx4 v252, s[42:43]\n"
"s_add_u32 s75, s74, 262144\n"
"v_add_u32 v252, s75, v240\n"
"s_add_u32 m0, s90, 33792\n"
"s_nop 0\n"
"global_load_lds_dwordx4 v252, s[42:43]\n"
"s_add_u32 s75, s74, 65536\n"
"v_add_u32 v252, s75, v240\n"
"s_add_u32 m0, s90, 34816\n"
"s_nop 0\n"
"global_load_lds_dwordx4 v252, s[42:43]\n"
"s_add_u32 s75, s74, 327680\n"
"v_add_u32 v252, s75, v240\n"
"s_add_u32 m0, s90, 35840\n"
"s_nop 0\n"
"global_load_lds_dwordx4 v252, s[42:43]\n"
"s_xor_b32 s74, s58, 2\n"
"s_lshl_b32 s74, s74, 20\n"
"s_add_u32 s75, s74, 0\n"
"v_add_u32 v252, s75, v240\n"
"s_add_u32 m0, s90, 36864\n"
"s_nop 0\n"
"global_load_lds_dwordx4 v252, s[44:45]\n"
"s_add_u32 s75, s74, 262144\n"
"v_add_u32 v252, s75, v240\n"
"s_add_u32 m0, s90, 37888\n"
"s_nop 0\n"
"global_load_lds_dwordx4 v252, s[44:45]\n"
"s_add_u32 s75, s74, 65536\n"
"v_add_u32 v252, s75, v240\n"
"s_add_u32 m0, s90, 38912\n"
"s_nop 0\n"
"global_load_lds_dwordx4 v252, s[44:45]\n"
"s_add_u32 s75, s74, 327680\n"
"v_add_u32 v252, s75, v240\n"
"s_add_u32 m0, s90, 39936\n"
"s_nop 0\n"
"global_load_lds_dwordx4 v252, s[44:45]\n"
"s_waitcnt vmcnt(36)\n"
"ds_read_b64_tr_b16 a[128:129], v242 offset:0\n"
"ds_read_b64_tr_b16 a[130:131], v242 offset:1024\n"
"ds_read_b64_tr_b16 a[132:133], v242 offset:2048\n"
"ds_read_b64_tr_b16 a[134:135], v242 offset:3072\n"
"ds_read_b64_tr_b16 a[192:193], v243 offset:0\n"
"ds_read_b64_tr_b16 a[194:195], v243 offset:1024\n"
"ds_read_b64_tr_b16 a[196:197], v243 offset:2048\n"
"ds_read_b64_tr_b16 a[198:199], v243 offset:3072\n"
"s_waitcnt lgkmcnt(0)\n"
"s_xor_b32 s74, s58, 2\n"
"s_lshl_b32 s74, s74, 20\n"
"s_add_u32 s75, s74, 524288\n"
"v_add_u32 v252, s75, v240\n"
"s_add_u32 m0, s90, 0\n"
"s_nop 0\n"
"global_load_lds_dwordx4 v252, s[42:43]\n"
"s_add_u32 s75, s74, 786432\n"
"v_add_u32 v252, s75, v240\n"
"s_add_u32 m0, s90, 1024\n"
"s_nop 0\n"
"global_load_lds_dwordx4 v252, s[42:43]\n"
"s_add_u32 s75, s74, 589824\n"
"v_add_u32 v252, s75, v240\n"
"s_add_u32 m0, s90, 2048\n"
"s_nop 0\n"
"global_load_lds_dwordx4 v252, s[42:43]\n"
"s_add_u32 s75, s74, 851968\n"
"v_add_u32 v252, s75, v240\n"
"s_add_u32 m0, s90, 3072\n"
"s_nop 0\n"
"global_load_lds_dwordx4 v252, s[42:43]\n"
"s_waitcnt vmcnt(36)\n"
"ds_read_b64_tr_b16 v[96:97], v242 offset:4096\n"
"ds_read_b64_tr_b16 v[98:99], v242 offset:5120\n"
"ds_read_b64_tr_b16 v[100:101], v242 offset:6144\n"
"ds_read_b64_tr_b16 v[102:103], v242 offset:7168\n"
"ds_read_b64_tr_b16 v[104:105], v243 offset:4096\n"
"ds_read_b64_tr_b16 v[106:107], v243 offset:5120\n"
"ds_read_b64_tr_b16 v[108:109], v243 offset:6144\n"
"ds_read_b64_tr_b16 v[110:111], v243 offset:7168\n"
"s_waitcnt lgkmcnt(0)\n"
"s_xor_b32 s74, s58, 2\n"
"s_lshl_b32 s74, s74, 20\n"
"s_add_u32 s75, s74, 524288\n"
"v_add_u32 v252, s75, v240\n"
"s_add_u32 m0, s90, 4096\n"
"s_nop 0\n"
"global_load_lds_dwordx4 v252, s[44:45]\n"
"s_add_u32 s75, s74, 786432\n"
"v_add_u32 v252, s75, v240\n"
"s_add_u32 m0, s90, 5120\n"
"s_nop 0\n"
"global_load_lds_dwordx4 v252, s[44:45]\n"
"s_add_u32 s75, s74, 589824\n"
"v_add_u32 v252, s75, v240\n"
"s_add_u32 m0, s90, 6144\n"
"s_nop 0\n"
"global_load_lds_dwordx4 v252, s[44:45]\n"
"s_add_u32 s75, s74, 851968\n"
"v_add_u32 v252, s75, v240\n"
"s_add_u32 m0, s90, 7168\n"
"s_nop 0\n"
"global_load_lds_dwordx4 v252, s[44:45]\n"
"s_waitcnt vmcnt(36)\n"
"ds_read_b64_tr_b16 a[136:137], v242 offset:8192\n"
"ds_read_b64_tr_b16 a[138:139], v242 offset:9216\n"
"ds_read_b64_tr_b16 a[140:141], v242 offset:10240\n"
"ds_read_b64_tr_b16 a[142:143], v242 offset:11264\n"
"ds_read_b64_tr_b16 a[200:201], v243 offset:8192\n"
"ds_read_b64_tr_b16 a[202:203], v243 offset:9216\n"
"ds_read_b64_tr_b16 a[204:205], v243 offset:10240\n"
"ds_read_b64_tr_b16 a[206:207], v243 offset:11264\n"
"s_waitcnt lgkmcnt(0)\n"
"s_xor_b32 s74, s58, 3\n"
"s_lshl_b32 s74, s74, 20\n"
"s_add_u32 s75, s74, 0\n"
"v_add_u32 v252, s75, v240\n"
"s_add_u32 m0, s90, 8192\n"
"s_nop 0\n"
"global_load_lds_dwordx4 v252, s[42:43]\n"
"s_add_u32 s75, s74, 262144\n"
"v_add_u32 v252, s75, v240\n"
"s_add_u32 m0, s90, 9216\n"
"s_nop 0\n"
"global_load_lds_dwordx4 v252, s[42:43]\n"
"s_add_u32 s75, s74, 65536\n"
"v_add_u32 v252, s75, v240\n"
"s_add_u32 m0, s90, 10240\n"
"s_nop 0\n"
"global_load_lds_dwordx4 v252, s[42:43]\n"
"s_add_u32 s75, s74, 327680\n"
"v_add_u32 v252, s75, v240\n"
"s_add_u32 m0, s90, 11264\n"
"s_nop 0\n"
"global_load_lds_dwordx4 v252, s[42:43]\n"
"s_waitcnt vmcnt(36)\n"
"ds_read_b64_tr_b16 v[112:113], v242 offset:12288\n"
"ds_read_b64_tr_b16 v[114:115], v242 offset:13312\n"
"ds_read_b64_tr_b16 v[116:117], v242 offset:14336\n"
"ds_read_b64_tr_b16 v[118:119], v242 offset:15360\n"
"ds_read_b64_tr_b16 v[120:121], v243 offset:12288\n"
"ds_read_b64_tr_b16 v[122:123], v243 offset:13312\n"
"ds_read_b64_tr_b16 v[124:125], v243 offset:14336\n"
"ds_read_b64_tr_b16 v[126:127], v243 offset:15360\n"
"s_waitcnt lgkmcnt(0)\n"
"s_xor_b32 s74, s58, 3\n"
"s_lshl_b32 s74, s74, 20\n"
"s_add_u32 s75, s74, 0\n"
"v_add_u32 v252, s75, v240\n"
"s_add_u32 m0, s90, 12288\n"
"s_nop 0\n"
"global_load_lds_dwordx4 v252, s[44:45]\n"
"s_add_u32 s75, s74, 262144\n"
"v_add_u32 v252, s75, v240\n"
"s_add_u32 m0, s90, 13312\n"
"s_nop 0\n"
"global_load_lds_dwordx4 v252, s[44:45]\n"
"s_add_u32 s75, s74, 65536\n"
"v_add_u32 v252, s75, v240\n"
"s_add_u32 m0, s90, 14336\n"
"s_nop 0\n"
"global_load_lds_dwordx4 v252, s[44:45]\n"
"s_add_u32 s75, s74, 327680\n"
"v_add_u32 v252, s75, v240\n"
"s_add_u32 m0, s90, 15360\n"
"s_nop 0\n"
"global_load_lds_dwordx4 v252, s[44:45]\n"
"s_waitcnt vmcnt(36)\n"
"ds_read_b64_tr_b16 a[144:145], v242 offset:16384\n"
"ds_read_b64_tr_b16 a[146:147], v242 offset:17408\n"
"ds_read_b64_tr_b16 a[148:149], v242 offset:18432\n"
"ds_read_b64_tr_b16 a[150:151], v242 offset:19456\n"
"ds_read_b64_tr_b16 a[208:209], v243 offset:16384\n"
"ds_read_b64_tr_b16 a[210:211], v243 offset:17408\n"
"ds_read_b64_tr_b16 a[212:213], v243 offset:18432\n"
"ds_read_b64_tr_b16 a[214:215], v243 offset:19456\n"
"s_waitcnt lgkmcnt(0)\n"
"s_xor_b32 s74, s58, 3\n"
"s_lshl_b32 s74, s74, 20\n"
"s_add_u32 s75, s74, 524288\n"
"v_add_u32 v252, s75, v240\n"
"s_add_u32 m0, s90, 16384\n"
"s_nop 0\n"
"global_load_lds_dwordx4 v252, s[42:43]\n"
"s_add_u32 s75, s74, 786432\n"
"v_add_u32 v252, s75, v240\n"
"s_add_u32 m0, s90, 17408\n"
"s_nop 0\n"
"global_load_lds_dwordx4 v252, s[42:43]\n"
"s_add_u32 s75, s74, 589824\n"
"v_add_u32 v252, s75, v240\n"
"s_add_u32 m0, s90, 18432\n"
"s_nop 0\n"
"global_load_lds_dwordx4 v252, s[42:43]\n"
"s_add_u32 s75, s74, 851968\n"
"v_add_u32 v252, s75, v240\n"
"s_add_u32 m0, s90, 19456\n"
"s_nop 0\n"
"global_load_lds_dwordx4 v252, s[42:43]\n"
"s_waitcnt vmcnt(36)\n"
"ds_read_b64_tr_b16 v[0:1], v242 offset:20480\n"
"ds_read_b64_tr_b16 v[2:3], v242 offset:21504\n"
"ds_read_b64_tr_b16 v[4:5], v242 offset:22528\n"
"ds_read_b64_tr_b16 v[6:7], v242 offset:23552\n"
"ds_read_b64_tr_b16 v[8:9], v243 offset:20480\n"
"ds_read_b64_tr_b16 v[10:11], v243 offset:21504\n"
"ds_read_b64_tr_b16 v[12:13], v243 offset:22528\n"
"ds_read_b64_tr_b16 v[14:15], v243 offset:23552\n"
"s_waitcnt lgkmcnt(0)\n"
"s_xor_b32 s74, s58, 3\n"
"s_lshl_b32 s74, s74, 20\n"
"s_add_u32 s75, s74, 524288\n"
"v_add_u32 v252, s75, v240\n"
"s_add_u32 m0, s90, 20480\n"
"s_nop 0\n"
"global_load_lds_dwordx4 v252, s[44:45]\n"
"s_add_u32 s75, s74, 786432\n"
"v_add_u32 v252, s75, v240\n"
"s_add_u32 m0, s90, 21504\n"
"s_nop 0\n"
"global_load_lds_dwordx4 v252, s[44:45]\n"
"s_add_u32 s75, s74, 589824\n"
"v_add_u32 v252, s75, v240\n"
"s_add_u32 m0, s90, 22528\n"
"s_nop 0\n"
"global_load_lds_dwordx4 v252, s[44:45]\n"
"s_add_u32 s75, s74, 851968\n"
"v_add_u32 v252, s75, v240\n"
"s_add_u32 m0, s90, 23552\n"
"s_nop 0\n"
"global_load_lds_dwordx4 v252, s[44:45]\n"
"s_waitcnt vmcnt(36)\n"
"ds_read_b64_tr_b16 a[152:153], v242 offset:24576\n"
"ds_read_b64_tr_b16 a[154:155], v242 offset:25600\n"
"ds_read_b64_tr_b16 a[156:157], v242 offset:26624\n"
"ds_read_b64_tr_b16 a[158:159], v242 offset:27648\n"
"ds_read_b64_tr_b16 a[216:217], v243 offset:24576\n"
"ds_read_b64_tr_b16 a[218:219], v243 offset:25600\n"
"ds_read_b64_tr_b16 a[220:221], v243 offset:26624\n"
"ds_read_b64_tr_b16 a[222:223], v243 offset:27648\n"
"s_waitcnt lgkmcnt(0)\n"
"s_xor_b32 s74, s58, 0\n"
"s_lshl_b32 s74, s74, 20\n"
"s_add_u32 s75, s74, 0\n"
"v_add_u32 v252, s75, v240\n"
"s_add_u32 m0, s90, 24576\n"
"s_nop 0\n"
"global_load_lds_dwordx4 v252, s[40:41]\n"
"s_add_u32 s75, s74, 262144\n"
"v_add_u32 v252, s75, v240\n"
"s_add_u32 m0, s90, 25600\n"
"s_nop 0\n"
"global_load_lds_dwordx4 v252, s[40:41]\n"
"s_add_u32 s75, s74, 65536\n"
"v_add_u32 v252, s75, v240\n"
"s_add_u32 m0, s90, 26624\n"
"s_nop 0\n"
"global_load_lds_dwordx4 v252, s[40:41]\n"
"s_add_u32 s75, s74, 327680\n"
"v_add_u32 v252, s75, v240\n"
"s_add_u32 m0, s90, 27648\n"
"s_nop 0\n"
"global_load_lds_dwordx4 v252, s[40:41]\n"
"s_waitcnt vmcnt(36)\n"
"ds_read_b64_tr_b16 v[16:17], v242 offset:28672\n"
"ds_read_b64_tr_b16 v[18:19], v242 offset:29696\n"
"ds_read_b64_tr_b16 v[20:21], v242 offset:30720\n"
"ds_read_b64_tr_b16 v[22:23], v242 offset:31744\n"
"ds_read_b64_tr_b16 v[24:25], v243 offset:28672\n"
"ds_read_b64_tr_b16 v[26:27], v243 offset:29696\n"
"ds_read_b64_tr_b16 v[28:29], v243 offset:30720\n"
"ds_read_b64_tr_b16 v[30:31], v243 offset:31744\n"
"s_waitcnt lgkmcnt(0)\n"
"s_xor_b32 s74, s58, 0\n"
"s_lshl_b32 s74, s74, 20\n"
"s_add_u32 s75, s74, 524288\n"
"v_add_u32 v252, s75, v240\n"
"s_add_u32 m0, s90, 28672\n"
"s_nop 0\n"
"global_load_lds_dwordx4 v252, s[40:41]\n"
"s_add_u32 s75, s74, 786432\n"
"v_add_u32 v252, s75, v240\n"
"s_add_u32 m0, s90, 29696\n"
"s_nop 0\n"
"global_load_lds_dwordx4 v252, s[40:41]\n"
"s_add_u32 s75, s74, 589824\n"
"v_add_u32 v252, s75, v240\n"
"s_add_u32 m0, s90, 30720\n"
"s_nop 0\n"
"global_load_lds_dwordx4 v252, s[40:41]\n"
"s_add_u32 s75, s74, 851968\n"
"v_add_u32 v252, s75, v240\n"
"s_add_u32 m0, s90, 31744\n"
"s_nop 0\n"
"global_load_lds_dwordx4 v252, s[40:41]\n"
"s_waitcnt vmcnt(36)\n"
"ds_read_b64_tr_b16 a[160:161], v242 offset:32768\n"
"ds_read_b64_tr_b16 a[162:163], v242 offset:33792\n"
"ds_read_b64_tr_b16 a[164:165], v242 offset:34816\n"
"ds_read_b64_tr_b16 a[166:167], v242 offset:35840\n"
"ds_read_b64_tr_b16 a[224:225], v243 offset:32768\n"
"ds_read_b64_tr_b16 a[226:227], v243 offset:33792\n"
"ds_read_b64_tr_b16 a[228:229], v243 offset:34816\n"
"ds_read_b64_tr_b16 a[230:231], v243 offset:35840\n"
"s_waitcnt lgkmcnt(0)\n"
"s_xor_b32 s74, s58, 1\n"
"s_lshl_b32 s74, s74, 20\n"
"s_add_u32 s75, s74, 0\n"
"v_add_u32 v252, s75, v240\n"
"s_add_u32 m0, s90, 32768\n"
"s_nop 0\n"
"global_load_lds_dwordx4 v252, s[40:41]\n"
"s_add_u32 s75, s74, 262144\n"
"v_add_u32 v252, s75, v240\n"
"s_add_u32 m0, s90, 33792\n"
"s_nop 0\n"
"global_load_lds_dwordx4 v252, s[40:41]\n"
"s_add_u32 s75, s74, 65536\n"
"v_add_u32 v252, s75, v240\n"
"s_add_u32 m0, s90, 34816\n"
"s_nop 0\n"
"global_load_lds_dwordx4 v252, s[40:41]\n"
"s_add_u32 s75, s74, 327680\n"
"v_add_u32 v252, s75, v240\n"
"s_add_u32 m0, s90, 35840\n"
"s_nop 0\n"
"global_load_lds_dwordx4 v252, s[40:41]\n"
"s_waitcnt vmcnt(36)\n"
"ds_read_b64_tr_b16 v[32:33], v242 offset:36864\n"
"ds_read_b64_tr_b16 v[34:35], v242 offset:37888\n"
"ds_read_b64_tr_b16 v[36:37], v242 offset:38912\n"
"ds_read_b64_tr_b16 v[38:39], v242 offset:39936\n"
"ds_read_b64_tr_b16 v[40:41], v243 offset:36864\n"
"ds_read_b64_tr_b16 v[42:43], v243 offset:37888\n"
"ds_read_b64_tr_b16 v[44:45], v243 offset:38912\n"
"ds_read_b64_tr_b16 v[46:47], v243 offset:39936\n"
"s_waitcnt lgkmcnt(0)\n"
"s_xor_b32 s74, s58, 1\n"
"s_lshl_b32 s74, s74, 20\n"
"s_add_u32 s75, s74, 524288\n"
"v_add_u32 v252, s75, v240\n"
"s_add_u32 m0, s90, 36864\n"
"s_nop 0\n"
"global_load_lds_dwordx4 v252, s[40:41]\n"
"s_add_u32 s75, s74, 786432\n"
"v_add_u32 v252, s75, v240\n"
"s_add_u32 m0, s90, 37888\n"
"s_nop 0\n"
"global_load_lds_dwordx4 v252, s[40:41]\n"
"s_add_u32 s75, s74, 589824\n"
"v_add_u32 v252, s75, v240\n"
"s_add_u32 m0, s90, 38912\n"
"s_nop 0\n"
"global_load_lds_dwordx4 v252, s[40:41]\n"
"s_add_u32 s75, s74, 851968\n"
"v_add_u32 v252, s75, v240\n"
"s_add_u32 m0, s90, 39936\n"
"s_nop 0\n"
"global_load_lds_dwordx4 v252, s[40:41]\n"
"s_waitcnt vmcnt(36)\n"
"ds_read_b64_tr_b16 a[168:169], v242 offset:0\n"
"ds_read_b64_tr_b16 a[170:171], v242 offset:1024\n"
"ds_read_b64_tr_b16 a[172:173], v242 offset:2048\n"
"ds_read_b64_tr_b16 a[174:175], v242 offset:3072\n"
"ds_read_b64_tr_b16 a[232:233], v243 offset:0\n"
"ds_read_b64_tr_b16 a[234:235], v243 offset:1024\n"
"ds_read_b64_tr_b16 a[236:237], v243 offset:2048\n"
"ds_read_b64_tr_b16 a[238:239], v243 offset:3072\n"
"s_waitcnt lgkmcnt(0)\n"
"s_xor_b32 s74, s58, 2\n"
"s_lshl_b32 s74, s74, 20\n"
"s_add_u32 s75, s74, 0\n"
"v_add_u32 v252, s75, v240\n"
"s_add_u32 m0, s90, 0\n"
"s_nop 0\n"
"global_load_lds_dwordx4 v252, s[40:41]\n"
"s_add_u32 s75, s74, 262144\n"
"v_add_u32 v252, s75, v240\n"
"s_add_u32 m0, s90, 1024\n"
"s_nop 0\n"
"global_load_lds_dwordx4 v252, s[40:41]\n"
"s_add_u32 s75, s74, 65536\n"
"v_add_u32 v252, s75, v240\n"
"s_add_u32 m0, s90, 2048\n"
"s_nop 0\n"
"global_load_lds_dwordx4 v252, s[40:41]\n"
"s_add_u32 s75, s74, 327680\n"
"v_add_u32 v252, s75, v240\n"
"s_add_u32 m0, s90, 3072\n"
"s_nop 0\n"
"global_load_lds_dwordx4 v252, s[40:41]\n"
"s_waitcnt vmcnt(36)\n"
"ds_read_b64_tr_b16 v[48:49], v242 offset:4096\n"
"ds_read_b64_tr_b16 v[50:51], v242 offset:5120\n"
"ds_read_b64_tr_b16 v[52:53], v242 offset:6144\n"
"ds_read_b64_tr_b16 v[54:55], v242 offset:7168\n"
"ds_read_b64_tr_b16 v[56:57], v243 offset:4096\n"
"ds_read_b64_tr_b16 v[58:59], v243 offset:5120\n"
"ds_read_b64_tr_b16 v[60:61], v243 offset:6144\n"
"ds_read_b64_tr_b16 v[62:63], v243 offset:7168\n"
"s_waitcnt lgkmcnt(0)\n"
"s_xor_b32 s74, s58, 2\n"
"s_lshl_b32 s74, s74, 20\n"
"s_add_u32 s75, s74, 524288\n"
"v_add_u32 v252, s75, v240\n"
"s_add_u32 m0, s90, 4096\n"
"s_nop 0\n"
"global_load_lds_dwordx4 v252, s[40:41]\n"
"s_add_u32 s75, s74, 786432\n"
"v_add_u32 v252, s75, v240\n"
"s_add_u32 m0, s90, 5120\n"
"s_nop 0\n"
"global_load_lds_dwordx4 v252, s[40:41]\n"
"s_add_u32 s75, s74, 589824\n"
"v_add_u32 v252, s75, v240\n"
"s_add_u32 m0, s90, 6144\n"
"s_nop 0\n"
"global_load_lds_dwordx4 v252, s[40:41]\n"
"s_add_u32 s75, s74, 851968\n"
"v_add_u32 v252, s75, v240\n"
"s_add_u32 m0, s90, 7168\n"
"s_nop 0\n"
"global_load_lds_dwordx4 v252, s[40:41]\n"
"s_waitcnt vmcnt(36)\n"
"ds_read_b64_tr_b16 a[176:177], v242 offset:8192\n"
"ds_read_b64_tr_b16 a[178:179], v242 offset:9216\n"
"ds_read_b64_tr_b16 a[180:181], v242 offset:10240\n"
"ds_read_b64_tr_b16 a[182:183], v242 offset:11264\n"
"ds_read_b64_tr_b16 a[240:241], v243 offset:8192\n"
"ds_read_b64_tr_b16 a[242:243], v243 offset:9216\n"
"ds_read_b64_tr_b16 a[244:245], v243 offset:10240\n"
"ds_read_b64_tr_b16 a[246:247], v243 offset:11264\n"
"s_waitcnt lgkmcnt(0)\n"
"s_xor_b32 s74, s58, 3\n"
"s_lshl_b32 s74, s74, 20\n"
"s_add_u32 s75, s74, 0\n"
"v_add_u32 v252, s75, v240\n"
"s_add_u32 m0, s90, 8192\n"
"s_nop 0\n"
"global_load_lds_dwordx4 v252, s[40:41]\n"
"s_add_u32 s75, s74, 262144\n"
"v_add_u32 v252, s75, v240\n"
"s_add_u32 m0, s90, 9216\n"
"s_nop 0\n"
"global_load_lds_dwordx4 v252, s[40:41]\n"
"s_add_u32 s75, s74, 65536\n"
"v_add_u32 v252, s75, v240\n"
"s_add_u32 m0, s90, 10240\n"
"s_nop 0\n"
"global_load_lds_dwordx4 v252, s[40:41]\n"
"s_add_u32 s75, s74, 327680\n"
"v_add_u32 v252, s75, v240\n"
"s_add_u32 m0, s90, 11264\n"
"s_nop 0\n"
"global_load_lds_dwordx4 v252, s[40:41]\n"
"s_waitcnt vmcnt(36)\n"
"ds_read_b64_tr_b16 v[64:65], v242 offset:12288\n"
"ds_read_b64_tr_b16 v[66:67], v242 offset:13312\n"
"ds_read_b64_tr_b16 v[68:69], v242 offset:14336\n"
"ds_read_b64_tr_b16 v[70:71], v242 offset:15360\n"
"ds_read_b64_tr_b16 v[72:73], v243 offset:12288\n"
"ds_read_b64_tr_b16 v[74:75], v243 offset:13312\n"
"ds_read_b64_tr_b16 v[76:77], v243 offset:14336\n"
"ds_read_b64_tr_b16 v[78:79], v243 offset:15360\n"
"s_waitcnt lgkmcnt(0)\n"
"s_xor_b32 s74, s58, 3\n"
"s_lshl_b32 s74, s74, 20\n"
"s_add_u32 s75, s74, 524288\n"
"v_add_u32 v252, s75, v240\n"
"s_add_u32 m0, s90, 12288\n"
"s_nop 0\n"
"global_load_lds_dwordx4 v252, s[40:41]\n"
"s_add_u32 s75, s74, 786432\n"
"v_add_u32 v252, s75, v240\n"
"s_add_u32 m0, s90, 13312\n"
"s_nop 0\n"
"global_load_lds_dwordx4 v252, s[40:41]\n"
"s_add_u32 s75, s74, 589824\n"
"v_add_u32 v252, s75, v240\n"
"s_add_u32 m0, s90, 14336\n"
"s_nop 0\n"
"global_load_lds_dwordx4 v252, s[40:41]\n"
"s_add_u32 s75, s74, 851968\n"
"v_add_u32 v252, s75, v240\n"
"s_add_u32 m0, s90, 15360\n"
"s_nop 0\n"
"global_load_lds_dwordx4 v252, s[40:41]\n"
"s_waitcnt vmcnt(36)\n"
"ds_read_b64_tr_b16 a[184:185], v242 offset:16384\n"
"ds_read_b64_tr_b16 a[186:187], v242 offset:17408\n"
"ds_read_b64_tr_b16 a[188:189], v242 offset:18432\n"
"ds_read_b64_tr_b16 a[190:191], v242 offset:19456\n"
"ds_read_b64_tr_b16 a[248:249], v243 offset:16384\n"
"ds_read_b64_tr_b16 a[250:251], v243 offset:17408\n"
"ds_read_b64_tr_b16 a[252:253], v243 offset:18432\n"
"ds_read_b64_tr_b16 a[254:255], v243 offset:19456\n"
"s_waitcnt lgkmcnt(0)\n"
"v_add_u32 v252, 0, v241\n"
"s_add_u32 m0, s90, 16384\n"
"s_nop 0\n"
"global_load_lds_dwordx4 v252, s[46:47]\n"
"v_add_u32 v252, 2048, v241\n"
"s_add_u32 m0, s90, 17408\n"
"s_nop 0\n"
"global_load_lds_dwordx4 v252, s[46:47]\n"
"v_add_u32 v252, 32768, v241\n"
"s_add_u32 m0, s90, 18432\n"
"s_nop 0\n"
"global_load_lds_dwordx4 v252, s[46:47]\n"
"v_add_u32 v252, 34816, v241\n"
"s_add_u32 m0, s90, 19456\n"
"s_nop 0\n"
"global_load_lds_dwordx4 v252, s[46:47]\n"
"s_waitcnt vmcnt(36)\n"
"ds_read_b64_tr_b16 v[80:81], v242 offset:20480\n"
"ds_read_b64_tr_b16 v[82:83], v242 offset:21504\n"
"ds_read_b64_tr_b16 v[84:85], v242 offset:22528\n"
"ds_read_b64_tr_b16 v[86:87], v242 offset:23552\n"
"ds_read_b64_tr_b16 v[88:89], v243 offset:20480\n"
"ds_read_b64_tr_b16 v[90:91], v243 offset:21504\n"
"ds_read_b64_tr_b16 v[92:93], v243 offset:22528\n"
"ds_read_b64_tr_b16 v[94:95], v243 offset:23552\n"
"s_waitcnt lgkmcnt(0)\n"
"v_add_u32 v252, 64, v241\n"
"s_add_u32 m0, s90, 20480\n"
"s_nop 0\n"
"global_load_lds_dwordx4 v252, s[46:47]\n"
"v_add_u32 v252, 2112, v241\n"
"s_add_u32 m0, s90, 21504\n"
"s_nop 0\n"
"global_load_lds_dwordx4 v252, s[46:47]\n"
"v_add_u32 v252, 32832, v241\n"
"s_add_u32 m0, s90, 22528\n"
"s_nop 0\n"
"global_load_lds_dwordx4 v252, s[46:47]\n"
"v_add_u32 v252, 34880, v241\n"
"s_add_u32 m0, s90, 23552\n"
"s_nop 0\n"
"global_load_lds_dwordx4 v252, s[46:47]\n"
"s_waitcnt vmcnt(36)\n"
"ds_read_b64_tr_b16 a[0:1], v242 offset:24576\n"
"ds_read_b64_tr_b16 a[2:3], v242 offset:25600\n"
"ds_read_b64_tr_b16 a[4:5], v242 offset:26624\n"
"ds_read_b64_tr_b16 a[6:7], v242 offset:27648\n"
"ds_read_b64_tr_b16 a[64:65], v243 offset:24576\n"
"ds_read_b64_tr_b16 a[66:67], v243 offset:25600\n"
"ds_read_b64_tr_b16 a[68:69], v243 offset:26624\n"
"ds_read_b64_tr_b16 a[70:71], v243 offset:27648\n"
"s_waitcnt lgkmcnt(0)\n"
"v_add_u32 v252, 65536, v241\n"
"s_add_u32 m0, s90, 24576\n"
"s_nop 0\n"
"global_load_lds_dwordx4 v252, s[46:47]\n"
"v_add_u32 v252, 67584, v241\n"
"s_add_u32 m0, s90, 25600\n"
"s_nop 0\n"
"global_load_lds_dwordx4 v252, s[46:47]\n"
"v_add_u32 v252, 98304, v241\n"
"s_add_u32 m0, s90, 26624\n"
"s_nop 0\n"
"global_load_lds_dwordx4 v252, s[46:47]\n"
"v_add_u32 v252, 100352, v241\n"
"s_add_u32 m0, s90, 27648\n"
"s_nop 0\n"
"global_load_lds_dwordx4 v252, s[46:47]\n"
"s_waitcnt vmcnt(36)\n"
"ds_read_b64_tr_b16 a[8:9], v242 offset:28672\n"
"ds_read_b64_tr_b16 a[10:11], v242 offset:29696\n"
"ds_read_b64_tr_b16 a[12:13], v242 offset:30720\n"
"ds_read_b64_tr_b16 a[14:15], v242 offset:31744\n"
"ds_read_b64_tr_b16 a[72:73], v243 offset:28672\n"
"ds_read_b64_tr_b16 a[74:75], v243 offset:29696\n"
"ds_read_b64_tr_b16 a[76:77], v243 offset:30720\n"
"ds_read_b64_tr_b16 a[78:79], v243 offset:31744\n"
"s_waitcnt lgkmcnt(0)\n"
"v_add_u32 v252, 65600, v241\n"
"s_add_u32 m0, s90, 28672\n"
"s_nop 0\n"
"global_load_lds_dwordx4 v252, s[46:47]\n"
"v_add_u32 v252, 67648, v241\n"
"s_add_u32 m0, s90, 29696\n"
"s_nop 0\n"
"global_load_lds_dwordx4 v252, s[46:47]\n"
"v_add_u32 v252, 98368, v241\n"
"s_add_u32 m0, s90, 30720\n"
"s_nop 0\n"
"global_load_lds_dwordx4 v252, s[46:47]\n"
"v_add_u32 v252, 100416, v241\n"
"s_add_u32 m0, s90, 31744\n"
"s_nop 0\n"
"global_load_lds_dwordx4 v252, s[46:47]\n"
"s_waitcnt vmcnt(36)\n"
"ds_read_b64_tr_b16 a[16:17], v242 offset:32768\n"
"ds_read_b64_tr_b16 a[18:19], v242 offset:33792\n"
"ds_read_b64_tr_b16 a[20:21], v242 offset:34816\n"
"ds_read_b64_tr_b16 a[22:23], v242 offset:35840\n"
"ds_read_b64_tr_b16 a[80:81], v243 offset:32768\n"
"ds_read_b64_tr_b16 a[82:83], v243 offset:33792\n"
"ds_read_b64_tr_b16 a[84:85], v243 offset:34816\n"
"ds_read_b64_tr_b16 a[86:87], v243 offset:35840\n"
"s_waitcnt vmcnt(32)\n"
"ds_read_b64_tr_b16 a[24:25], v242 offset:36864\n"
"ds_read_b64_tr_b16 a[26:27], v242 offset:37888\n"
"ds_read_b64_tr_b16 a[28:29], v242 offset:38912\n"
"ds_read_b64_tr_b16 a[30:31], v242 offset:39936\n"
"ds_read_b64_tr_b16 a[88:89], v243 offset:36864\n"
"ds_read_b64_tr_b16 a[90:91], v243 offset:37888\n"
"ds_read_b64_tr_b16 a[92:93], v243 offset:38912\n"
"ds_read_b64_tr_b16 a[94:95], v243 offset:39936\n"
"s_waitcnt vmcnt(28)\n"
"ds_read_b64_tr_b16 a[32:33], v242 offset:0\n"
"ds_read_b64_tr_b16 a[34:35], v242 offset:1024\n"
"ds_read_b64_tr_b16 a[36:37], v242 offset:2048\n"
"ds_read_b64_tr_b16 a[38:39], v242 offset:3072\n"
"ds_read_b64_tr_b16 a[96:97], v243 offset:0\n"
"ds_read_b64_tr_b16 a[98:99], v243 offset:1024\n"
"ds_read_b64_tr_b16 a[100:101], v243 offset:2048\n"
"ds_read_b64_tr_b16 a[102:103], v243 offset:3072\n"
"s_waitcnt vmcnt(24)\n"
"ds_read_b64_tr_b16 a[40:41], v242 offset:4096\n"
"ds_read_b64_tr_b16 a[42:43], v242 offset:5120\n"
"ds_read_b64_tr_b16 a[44:45], v242 offset:6144\n"
"ds_read_b64_tr_b16 a[46:47], v242 offset:7168\n"
"ds_read_b64_tr_b16 a[104:105], v243 offset:4096\n"
"ds_read_b64_tr_b16 a[106:107], v243 offset:5120\n"
"ds_read_b64_tr_b16 a[108:109], v243 offset:6144\n"
"ds_read_b64_tr_b16 a[110:111], v243 offset:7168\n"
"s_waitcnt vmcnt(20)\n"
"ds_read_b64_tr_b16 a[48:49], v242 offset:8192\n"
"ds_read_b64_tr_b16 a[50:51], v242 offset:9216\n"
"ds_read_b64_tr_b16 a[52:53], v242 offset:10240\n"
"ds_read_b64_tr_b16 a[54:55], v242 offset:11264\n"
"ds_read_b64_tr_b16 a[112:113], v243 offset:8192\n"
"ds_read_b64_tr_b16 a[114:115], v243 offset:9216\n"
"ds_read_b64_tr_b16 a[116:117], v243 offset:10240\n"
"ds_read_b64_tr_b16 a[118:119], v243 offset:11264\n"
"s_waitcnt vmcnt(16)\n"
"ds_read_b64_tr_b16 a[56:57], v242 offset:12288\n"
"ds_read_b64_tr_b16 a[58:59], v242 offset:13312\n"
"ds_read_b64_tr_b16 a[60:61], v242 offset:14336\n"
"ds_read_b64_tr_b16 a[62:63], v242 offset:15360\n"
"ds_read_b64_tr_b16 a[120:121], v243 offset:12288\n"
"ds_read_b64_tr_b16 a[122:123], v243 offset:13312\n"
"ds_read_b64_tr_b16 a[124:125], v243 offset:14336\n"
"ds_read_b64_tr_b16 a[126:127], v243 offset:15360\n"
"s_waitcnt vmcnt(12)\n"
"ds_read_b64_tr_b16 v[128:129], v242 offset:16384\n"
"ds_read_b64_tr_b16 v[130:131], v242 offset:17408\n"
"ds_read_b64_tr_b16 v[132:133], v242 offset:18432\n"
"ds_read_b64_tr_b16 v[134:135], v242 offset:19456\n"
"ds_read_b64_tr_b16 v[136:137], v243 offset:16384\n"
"ds_read_b64_tr_b16 v[138:139], v243 offset:17408\n"
"ds_read_b64_tr_b16 v[140:141], v243 offset:18432\n"
"ds_read_b64_tr_b16 v[142:143], v243 offset:19456\n"
"s_waitcnt vmcnt(8)\n"
"ds_read_b64_tr_b16 v[144:145], v242 offset:20480\n"
"ds_read_b64_tr_b16 v[146:147], v242 offset:21504\n"
"ds_read_b64_tr_b16 v[148:149], v242 offset:22528\n"
"ds_read_b64_tr_b16 v[150:151], v242 offset:23552\n"
"ds_read_b64_tr_b16 v[152:153], v243 offset:20480\n"
"ds_read_b64_tr_b16 v[154:155], v243 offset:21504\n"
"ds_read_b64_tr_b16 v[156:157], v243 offset:22528\n"
"ds_read_b64_tr_b16 v[158:159], v243 offset:23552\n"
"s_waitcnt vmcnt(4)\n"
"ds_read_b64_tr_b16 v[160:161], v242 offset:24576\n"
"ds_read_b64_tr_b16 v[162:163], v242 offset:25600\n"
"ds_read_b64_tr_b16 v[164:165], v242 offset:26624\n"
"ds_read_b64_tr_b16 v[166:167], v242 offset:27648\n"
"ds_read_b64_tr_b16 v[168:169], v243 offset:24576\n"
"ds_read_b64_tr_b16 v[170:171], v243 offset:25600\n"
"ds_read_b64_tr_b16 v[172:173], v243 offset:26624\n"
"ds_read_b64_tr_b16 v[174:175], v243 offset:27648\n"
"s_waitcnt vmcnt(0)\n"
"ds_read_b64_tr_b16 v[176:177], v242 offset:28672\n"
"ds_read_b64_tr_b16 v[178:179], v242 offset:29696\n"
"ds_read_b64_tr_b16 v[180:181], v242 offset:30720\n"
"ds_read_b64_tr_b16 v[182:183], v242 offset:31744\n"
"ds_read_b64_tr_b16 v[184:185], v243 offset:28672\n"
"ds_read_b64_tr_b16 v[186:187], v243 offset:29696\n"
"ds_read_b64_tr_b16 v[188:189], v243 offset:30720\n"
"ds_read_b64_tr_b16 v[190:191], v243 offset:31744\n"
"s_waitcnt lgkmcnt(0)\n"
"s_barrier\n"
"ds_write_b128 v236, v[96:99] offset:0\n"
"ds_write_b128 v236, v[100:103] offset:1024\n"
"ds_write_b128 v236, v[104:107] offset:2048\n"
"ds_write_b128 v236, v[108:111] offset:3072\n"
"ds_write_b128 v236, v[112:115] offset:4096\n"
"ds_write_b128 v236, v[116:119] offset:5120\n"
"ds_write_b128 v236, v[120:123] offset:6144\n"
"ds_write_b128 v236, v[124:127] offset:7168\n"
"s_waitcnt lgkmcnt(4)\n"
"ds_write_b128 v236, v[128:131] offset:8192\n"
"ds_write_b128 v236, v[132:135] offset:9216\n"
"ds_write_b128 v236, v[136:139] offset:10240\n"
"ds_write_b128 v236, v[140:143] offset:11264\n"
"ds_write_b128 v236, v[144:147] offset:12288\n"
"ds_write_b128 v236, v[148:151] offset:13312\n"
"ds_write_b128 v236, v[152:155] offset:14336\n"
"ds_write_b128 v236, v[156:159] offset:15360\n"
"s_waitcnt lgkmcnt(4)\n"
"ds_write_b128 v236, v[160:163] offset:16384\n"
"ds_write_b128 v236, v[164:167] offset:17408\n"
"ds_write_b128 v236, v[168:171] offset:18432\n"
"ds_write_b128 v236, v[172:175] offset:19456\n"
"ds_write_b128 v236, v[176:179] offset:20480\n"
"ds_write_b128 v236, v[180:183] offset:21504\n"
"ds_write_b128 v236, v[184:187] offset:22528\n"
"ds_write_b128 v236, v[188:191] offset:23552\n"
"s_waitcnt lgkmcnt(4)\n"
"ds_write_b128 v225, v[244:247] offset:31296\n"
"s_waitcnt lgkmcnt(0)\n"
"v_mov_b32 v240, 0x3f803f80\n"
"v_mov_b32 v241, 0x3f803f80\n"
"v_mov_b32 v242, 0x3f803f80\n"
"v_mov_b32 v243, 0x3f803f80\n"
"v_mov_b32 v244, 0\n"
"v_mov_b32 v245, 0\n"
"v_mov_b32 v246, 0\n"
"v_mov_b32 v247, 0\n"
"v_mov_b32 v248, 0\n"
"v_mov_b32 v249, 0\n"
"s_mov_b32 s66, 0\n"
"s_mov_b32 s67, 0\n"
"s_mov_b32 s68, 0x40000\n"
"s_lshl_b32 s90, s58, 12\n"
"s_add_u32 s90, s90, 8192\n"
"s_sub_u32 s4, 0, s59\n"
"s_mov_b32 s5, s4\n"
"s_add_u32 s86, s48, 786432\n"
"s_addc_u32 s87, s49, 0\n"
"s_cmp_eq_u32 s58, 0\n"
"s_cbranch_scc0 .Lar_STARTB_%=\n"
"s_setprio 3\n"
"s_mov_b32 s50, 0x55555555\n"
"s_mov_b32 s51, 0x55555555\n"
"ds_read_b128 v[96:99], v236 offset:0\n"
"ds_read_b128 v[100:103], v236 offset:1024\n"
"ds_read_b128 v[104:107], v236 offset:2048\n"
"ds_read_b128 v[108:111], v236 offset:3072\n"
"ds_read_b128 v[112:115], v236 offset:4096\n"
"ds_read_b128 v[116:119], v236 offset:5120\n"
"ds_read_b128 v[120:123], v236 offset:6144\n"
"ds_read_b128 v[124:127], v236 offset:7168\n"
"v_mov_b64 v[200:201], 0\n"
"v_mov_b64 v[202:203], 0\n"
"v_mov_b64 v[204:205], 0\n"
"v_mov_b64 v[206:207], 0\n"
"v_mov_b64 v[208:209], 0\n"
"v_mov_b64 v[210:211], 0\n"
"v_mov_b64 v[212:213], 0\n"
"v_mov_b64 v[214:215], 0\n"
"v_mov_b64 v[216:217], 0\n"
"v_mov_b64 v[218:219], 0\n"
"v_mov_b64 v[220:221], 0\n"
"v_mov_b64 v[222:223], 0\n"
"s_and_b32 s74, s66, 3\n"
"s_lshl2_add_u32 s74, s74, s71\n"
"s_add_u32 s74, s74, 32\n"
"s_lshl_b32 s74, s74, 14\n"
"s_add_u32 s76, s48, s74\n"
"s_addc_u32 s77, s49, 0\n"
"s_andn2_b32 s74, 1, s66\n"
"s_lshl_b32 s74, s74, 20\n"
"s_add_u32 s74, s74, s91\n"
"s_add_u32 s40, s48, s74\n"
"s_addc_u32 s41, s49, 0\n"
"s_add_u32 s74, s66, 3\n"
"s_and_b32 s74, s74, 3\n"
"s_lshl_b32 s74, s74, 16\n"
"s_add_u32 s74, s74, 524288\n"
"s_add_u32 s84, s48, s74\n"
"s_addc_u32 s85, s49, 0\n"
"s_and_b32 s74, s66, 1\n"
"s_lshl_b32 s74, s74, 20\n"
"s_add_u32 s78, s48, s74\n"
"s_addc_u32 s79, s49, 0\n"
"global_load_dwordx4 v[160:163], v237, s[76:77] sc1\n"
"global_load_dwordx4 v[164:167], v237, s[76:77] offset:1024 sc1\n"
"s_waitcnt vmcnt(0) lgkmcnt(0)\n"
"s_barrier\n"
"ds_read_b128 v[128:131], v225 offset:8192\n"
"ds_read_b128 v[132:135], v225 offset:9216\n"
"ds_read_b128 v[136:139], v225 offset:10240\n"
"ds_read_b128 v[140:143], v225 offset:11264\n"
"ds_read_b128 v[144:147], v225 offset:12288\n"
"ds_read_b128 v[148:151], v225 offset:13312\n"
"ds_read_b128 v[152:155], v225 offset:14336\n"
"ds_read_b128 v[156:159], v225 offset:15360\n"
"ds_read_b128 v[192:195], v226 offset:16384\n"
"ds_read_b128 v[196:199], v226 offset:17408\n"
"v_mov_b64 v[168:169], v[160:161]\n"
"v_mov_b64 v[170:171], v[162:163]\n"
"v_mov_b64 v[172:173], v[164:165]\n"
"v_mov_b64 v[174:175], v[166:167]\n"
"s_waitcnt lgkmcnt(0)\n"
"s_memtime s[42:43]\n"
"s_memrealtime s[44:45]\n"
"s_nop 2\n"
"s_memrealtime s[46:47]\n"
"s_nop 2\n"
"s_memrealtime s[98:99]\n"
"s_nop 2\n"
"s_memrealtime s[96:97]\n"
"s_nop 2\n"
"s_memrealtime s[94:95]\n"
"s_nop 2\n"
"s_memrealtime s[92:93]\n"
"s_waitcnt lgkmcnt(0)\n"
"s_add_u32 s44, s44, s46\n"
"s_add_u32 s44, s44, s98\n"
"s_add_u32 s44, s44, s96\n"
"s_add_u32 s44, s44, s94\n"
"s_add_u32 s44, s44, s92\n"
"s_lshl_b32 s44, s44, 2\n"
"s_sub_u32 s74, s42, s56\n"
"s_sub_u32 s75, s44, s67\n"
"s_mov_b32 s56, s42\n"
"s_mov_b32 s67, s44\n"
"s_mov_b32 s88, 68000\n"
"s_mov_b32 s89, 63161\n"
"s_movk_i32 s90, 1800\n"
"s_mov_b32 s36, 0x00ff00ff\n"
"s_mov_b32 s37, 0x00ff00ff\n"
".p2align 6\n"
".Lar_LOOPA_%=:\n"
"s_waitcnt vmcnt(3)\n"
"ds_write_b128 v226, v[160:163]\n"
"s_waitcnt vmcnt(2)\n"
"ds_write_b128 v226, v[164:167] offset:1024\n"
"v_smfmac_f32_16x16x64_bf16 v[200:203], v[160:163], a[0:7], v224\n"
"v_cmp_u_f32 vcc, v160, v161\n"
"v_smfmac_f32_16x16x64_bf16 v[204:207], v[160:163], a[64:71], v224\n"
"v_cmp_u_f32 s[72:73], v162, v163\n"
"v_smfmac_f32_16x16x64_bf16 v[208:211], v[160:163], a[128:135], v224\n"
"s_or_b64 vcc, vcc, s[72:73]\n"
"v_cmp_u_f32 s[42:43], v164, v165\n"
"v_smfmac_f32_16x16x64_bf16 v[212:215], v[160:163], a[192:199], v224\n"
"s_or_b64 vcc, vcc, s[42:43]\n"
"v_cmp_u_f32 s[74:75], v166, v167\n"
"v_smfmac_f32_16x16x64_bf16 v[200:203], v[164:167], a[8:15], v224\n"
"s_or_b64 vcc, vcc, s[74:75]\n"
"s_cbranch_vccnz .Lar_MSLOWA_%=\n"
".Lar_MOKA_%=:\n"
"v_smfmac_f32_16x16x64_bf16 v[216:219], v[160:163], v[96:103], v224\n"
"v_smfmac_f32_16x16x64_bf16 v[220:223], v[160:163], v[104:111], v224\n"
"s_waitcnt vmcnt(0) lgkmcnt(0)\n"
"s_barrier\n"
"ds_read_b128 v[168:171], v227 offset:0\n"
"s_memtime s[72:73]\n"
"ds_read_b128 v[172:175], v227 offset:1024\n"
"v_smfmac_f32_16x16x64_bf16 v[204:207], v[164:167], a[72:79], v224\n"
"ds_read_b128 v[176:179], v228 offset:0\n"
"ds_read_b128 v[180:183], v228 offset:1024\n"
"ds_read_b128 v[184:187], v229 offset:0\n"
"v_smfmac_f32_16x16x64_bf16 v[208:211], v[164:167], a[136:143], v224\n"
"ds_read_b128 v[188:191], v229 offset:1024\n"
"s_lshl_b32 s74, s60, 7\n"
"v_smfmac_f32_16x16x64_bf16 v[212:215], v[164:167], a[200:207], v224\n"
"v_mov_b32 v255, s74\n"
"v_smfmac_f32_16x16x64_bf16 v[216:219], v[164:167], v[112:119], v224\n"
"s_waitcnt lgkmcnt(5)\n"
"v_smfmac_f32_16x16x64_bf16 v[200:203], v[168:171], a[16:23], v224\n"
"s_waitcnt lgkmcnt(4)\n"
"v_smfmac_f32_16x16x64_bf16 v[200:203], v[172:175], a[24:31], v224\n"
"s_sub_u32 s38, s72, s56\n"
"s_add_u32 s38, s38, s90\n"
"s_waitcnt lgkmcnt(3)\n"
"v_mov_b32 v160, 0x7f817f81\n"
"v_smfmac_f32_16x16x64_bf16 v[200:203], v[176:179], a[32:39], v224\n"
"s_min_u32 s38, s38, 0x7000\n"
"s_mul_i32 s38, s38, s88\n"
"s_waitcnt lgkmcnt(2)\n"
"v_smfmac_f32_16x16x64_bf16 v[200:203], v[180:183], a[40:47], v224\n"
"s_lshr_b32 s38, s38, 16\n"
"s_add_u32 s69, s38, s67\n"
"s_waitcnt lgkmcnt(1)\n"
"v_mov_b32 v161, 0x7f817f81\n"
"v_smfmac_f32_16x16x64_bf16 v[200:203], v[184:187], a[48:55], v224\n"
"s_lshl_b32 s69, s69, 6\n"
"s_or_b32 s69, s69, s66\n"
"s_waitcnt lgkmcnt(0)\n"
"v_smfmac_f32_16x16x64_bf16 v[200:203], v[188:191], a[56:63], v224\n"
"s_mov_b32 s57, s72\n"
"v_mov_b32 v254, s69\n"
"v_smfmac_f32_16x16x64_bf16 v[204:207], v[168:171], a[80:87], v224\n"
"s_mov_b64 exec, 1\n"
"global_store_dword v255, v254, s[86:87] sc1\n"
"s_mov_b64 exec, -1\n"
"v_smfmac_f32_16x16x64_bf16 v[204:207], v[172:175], a[88:95], v224\n"
"v_mov_b32 v162, 0x7f817f81\n"
"v_smfmac_f32_16x16x64_bf16 v[204:207], v[176:179], a[96:103], v224\n"
"v_mov_b32 v163, 0x7f817f81\n"
"v_smfmac_f32_16x16x64_bf16 v[204:207], v[180:183], a[104:111], v224\n"
"global_store_dwordx4 v225, v[160:163], s[40:41] sc1\n"
"v_smfmac_f32_16x16x64_bf16 v[204:207], v[184:187], a[112:119], v224\n"
"v_add_f32 v200, v200, v202\n"
"v_smfmac_f32_16x16x64_bf16 v[204:207], v[188:191], a[120:127], v224\n"
"s_nop 1\n"
"v_add_f32 v201, v201, v203\n"
"v_smfmac_f32_16x16x64_bf16 v[208:211], v[168:171], a[144:151], v224\n"
"s_nop 2\n"
"v_smfmac_f32_16x16x64_bf16 v[208:211], v[172:175], a[152:159], v224\n"
"v_add_f32 v204, v204, v206\n"
"v_smfmac_f32_16x16x64_bf16 v[208:211], v[176:179], a[160:167], v224\n"
"v_add_f32 v205, v205, v207\n"
"v_smfmac_f32_16x16x64_bf16 v[208:211], v[180:183], a[168:175], v224\n"
"v_cvt_pk_bf16_f32 v200, v200, v204\n"
"v_smfmac_f32_16x16x64_bf16 v[208:211], v[184:187], a[176:183], v224\n"
"v_cvt_pk_bf16_f32 v201, v201, v205\n"
"v_smfmac_f32_16x16x64_bf16 v[208:211], v[188:191], a[184:191], v224\n"
"ds_write2_b32 v230, v200, v201 offset0:0 offset1:72\n"
"s_nop 1\n"
"v_smfmac_f32_16x16x64_bf16 v[212:215], v[168:171], a[208:215], v224\n"
"s_mov_b64 exec, s[50:51]\n"
"global_store_dwordx4 v239, v[160:163], s[84:85] sc1\n"
"s_mov_b64 exec, -1\n"
"v_smfmac_f32_16x16x64_bf16 v[212:215], v[172:175], a[216:223], v224\n"
"s_nop 1\n"
"v_add_f32 v208, v208, v210\n"
"v_smfmac_f32_16x16x64_bf16 v[212:215], v[176:179], a[224:231], v224\n"
"v_add_f32 v209, v209, v211\n"
"v_smfmac_f32_16x16x64_bf16 v[212:215], v[180:183], a[232:239], v224\n"
"s_waitcnt lgkmcnt(0)\n"
"s_barrier\n"
"v_smfmac_f32_16x16x64_bf16 v[212:215], v[184:187], a[240:247], v224\n"
"ds_read_b128 v[96:99], v232 offset:0\n"
"ds_read_b128 v[100:103], v232 offset:64\n"
"ds_read_b128 v[104:107], v232 offset:128\n"
"v_smfmac_f32_16x16x64_bf16 v[212:215], v[188:191], a[248:255], v224\n"
"ds_read_b128 v[108:111], v232 offset:192\n"
"s_nop 1\n"
"v_smfmac_f32_16x16x64_bf16 v[220:223], v[164:167], v[120:127], v224\n"
"s_nop 3\n"
"v_add_f32 v212, v212, v214\n"
"v_add_f32 v213, v213, v215\n"
"v_cvt_pk_bf16_f32 v208, v208, v212\n"
"v_cvt_pk_bf16_f32 v209, v209, v213\n"
"ds_write2_b32 v231, v208, v209 offset0:0 offset1:72\n"
"s_waitcnt lgkmcnt(1)\n"
"v_mfma_f32_16x16x32_bf16 v[200:203], v[128:131], v[96:99], 0\n"
"v_mfma_f32_16x16x32_bf16 v[204:207], v[144:147], v[96:99], 0\n"
"v_mfma_f32_16x16x32_bf16 v[200:203], v[132:135], v[100:103], v[200:203]\n"
"v_mfma_f32_16x16x32_bf16 v[204:207], v[148:151], v[100:103], v[204:207]\n"
"s_waitcnt lgkmcnt(0)\n"
"s_barrier\n"
"ds_read_b128 v[112:115], v233 offset:0\n"
"ds_read_b128 v[116:119], v233 offset:64\n"
"v_mfma_f32_16x16x32_bf16 v[200:203], v[136:139], v[104:107], v[200:203]\n"
"ds_read_b128 v[120:123], v233 offset:128\n"
"ds_read_b128 v[124:127], v233 offset:192\n"
"v_mfma_f32_16x16x32_bf16 v[204:207], v[152:155], v[104:107], v[204:207]\n"
"v_lshlrev_b32 v255, 3, v225\n"
"v_mfma_f32_16x16x32_bf16 v[200:203], v[140:143], v[108:111], v[200:203]\n"
"v_mfma_f32_16x16x32_bf16 v[204:207], v[156:159], v[108:111], v[204:207]\n"
"s_waitcnt lgkmcnt(3)\n"
"v_mfma_f32_16x16x32_bf16 v[208:211], v[112:115], v[96:99], 0\n"
"ds_read_b128 v[128:131], v236 offset:8192\n"
"s_waitcnt lgkmcnt(3)\n"
"v_mfma_f32_16x16x32_bf16 v[208:211], v[116:119], v[100:103], v[208:211]\n"
"ds_read_b128 v[132:135], v236 offset:9216\n"
"s_waitcnt lgkmcnt(3)\n"
"v_mfma_f32_16x16x32_bf16 v[208:211], v[120:123], v[104:107], v[208:211]\n"
"ds_read_b128 v[136:139], v236 offset:10240\n"
"v_max3_f32 v212, v200, v201, v202\n"
"s_waitcnt lgkmcnt(3)\n"
"v_mfma_f32_16x16x32_bf16 v[208:211], v[124:127], v[108:111], v[208:211]\n"
"v_max3_f32 v212, v212, v203, v204\n"
"v_max3_f32 v212, v212, v205, v206\n"
"v_smfmac_f32_16x16x64_bf16 v[216:219], v[168:171], v[0:7], v224\n"
"ds_read_b128 v[140:143], v236 offset:11264\n"
"v_max_f32 v212, v212, v207\n"
"v_smfmac_f32_16x16x64_bf16 v[216:219], v[172:175], v[16:23], v224\n"
"ds_read_b128 v[144:147], v236 offset:12288\n"
"ds_read_b128 v[148:151], v236 offset:13312\n"
"v_max3_f32 v213, v212, v208, v209\n"
"v_max3_f32 v212, v212, v208, v209\n"
"v_smfmac_f32_16x16x64_bf16 v[216:219], v[176:179], v[32:39], v224\n"
"ds_read_b128 v[152:155], v236 offset:14336\n"
"v_permlane32_swap_b32 v212, v213\n"
"v_max_f32 v214, v212, v213\n"
"v_max_f32 v212, v212, v213\n"
"v_smfmac_f32_16x16x64_bf16 v[216:219], v[180:183], v[48:55], v224\n"
"ds_read_b128 v[156:159], v236 offset:15360\n"
"v_permlane16_swap_b32 v212, v214\n"
"v_max_f32 v212, v212, v214\n"
"v_mul_f32 v214, s63, v212\n"
"v_fma_f32 v200, v200, s63, -v214\n"
"v_smfmac_f32_16x16x64_bf16 v[216:219], v[184:187], v[64:71], v224\n"
"ds_read_b128 v[96:99], v236 offset:16384\n"
"v_exp_f32 v200, v200\n"
"v_fma_f32 v201, v201, s63, -v214\n"
"v_smfmac_f32_16x16x64_bf16 v[216:219], v[188:191], v[80:87], v224\n"
"ds_read_b128 v[100:103], v236 offset:17408\n"
"v_exp_f32 v201, v201\n"
"v_fma_f32 v202, v202, s63, -v214\n"
"v_smfmac_f32_16x16x64_bf16 v[220:223], v[168:171], v[8:15], v224\n"
"ds_read_b128 v[104:107], v236 offset:18432\n"
"v_exp_f32 v202, v202\n"
"v_fma_f32 v203, v203, s63, -v214\n"
"v_smfmac_f32_16x16x64_bf16 v[220:223], v[172:175], v[24:31], v224\n"
"ds_read_b128 v[108:111], v236 offset:19456\n"
"ds_read_b64 v[252:253], v251\n"
"v_exp_f32 v203, v203\n"
"v_fma_f32 v204, v204, s63, -v214\n"
"v_smfmac_f32_16x16x64_bf16 v[220:223], v[176:179], v[40:47], v224\n"
"ds_read_b128 v[112:115], v236 offset:20480\n"
"v_exp_f32 v204, v204\n"
"v_fma_f32 v205, v205, s63, -v214\n"
"v_smfmac_f32_16x16x64_bf16 v[220:223], v[180:183], v[56:63], v224\n"
"ds_read_b128 v[116:119], v236 offset:21504\n"
"v_exp_f32 v205, v205\n"
"v_fma_f32 v206, v206, s63, -v214\n"
"v_smfmac_f32_16x16x64_bf16 v[220:223], v[184:187], v[72:79], v224\n"
"ds_read_b128 v[120:123], v236 offset:22528\n"
"v_exp_f32 v206, v206\n"
"v_fma_f32 v207, v207, s63, -v214\n"
"v_smfmac_f32_16x16x64_bf16 v[220:223], v[188:191], v[88:95], v224\n"
"ds_read_b128 v[124:127], v236 offset:23552\n"
"global_load_dword v250, v255, s[86:87] sc1\n"
"v_exp_f32 v207, v207\n"
"v_fma_f32 v208, v208, s63, -v214\n"
"v_fma_f32 v209, v209, s63, -v214\n"
"v_cvt_pk_bf16_f32 v160, v200, v201\n"
"v_cvt_pk_bf16_f32 v161, v202, v203\n"
"v_cvt_pk_bf16_f32 v162, v204, v205\n"
"v_cvt_pk_bf16_f32 v163, v206, v207\n"
"v_exp_f32 v208, v208\n"
"v_exp_f32 v209, v209\n"
"v_mfma_f32_16x16x32_bf16 v[168:171], v[240:243], v[160:163], 0\n"
"v_cvt_pk_bf16_f32 v246, v208, v209\n"
"v_add_f32 v216, v216, v218\n"
"v_mfma_f32_16x16x32_bf16 v[172:175], v[192:195], v[160:163], 0\n"
"v_add_f32 v217, v217, v219\n"
"v_add_f32 v220, v220, v222\n"
"s_nop 0\n"
"v_mfma_f32_16x16x16_bf16 v[168:171], v[240:241], v[246:247], v[168:171]\n"
"v_add_f32 v221, v221, v223\n"
"v_cvt_pk_bf16_f32 v244, v216, v217\n"
"v_mfma_f32_16x16x32_bf16 v[176:179], v[196:199], v[160:163], 0\n"
"v_cvt_pk_bf16_f32 v245, v220, v221\n"
"v_cvt_pk_bf16_f32 v249, v208, v209\n"
"s_nop 0\n"
"v_mfma_f32_16x16x16_bf16 v[172:175], v[244:245], v[246:247], v[172:175]\n"
"s_nop 0\n"
"v_rcp_f32 v168, v168\n"
"v_mfma_f32_16x16x16_bf16 v[176:179], v[244:245], v[248:249], v[176:179]\n"
"s_waitcnt lgkmcnt(4)\n"
"v_cndmask_b32 v252, v252, v244, s[4:5]\n"
"v_cndmask_b32 v253, v253, v245, s[4:5]\n"
"v_lshrrev_b32 v254, 1, v225\n"
"s_mov_b64 exec, s[36:37]\n"
"v_pk_mul_f32 v[172:173], v[172:173], v[168:169] op_sel_hi:[1,0]\n"
"v_pk_mul_f32 v[174:175], v[174:175], v[168:169] op_sel_hi:[1,0]\n"
"s_nop 0\n"
"v_pk_mul_f32 v[176:177], v[176:177], v[168:169] op_sel_hi:[1,0]\n"
"v_pk_mul_f32 v[178:179], v[178:179], v[168:169] op_sel_hi:[1,0]\n"
"v_cvt_pk_bf16_f32 v164, v172, v173\n"
"v_cvt_pk_bf16_f32 v165, v174, v175\n"
"v_cvt_pk_bf16_f32 v166, v176, v177\n"
"v_cvt_pk_bf16_f32 v167, v178, v179\n"
"ds_write_b128 v234, v[164:167]\n"
"s_mov_b64 exec, -1\n"
"v_mfma_f32_32x32x8_bf16 v[200:215], v[246:247], v[248:249], 0\n"
"global_store_dwordx2 v254, v[252:253], s[52:53] sc1\n"
"s_cmp_eq_u32 s66, 63\n"
"s_cbranch_scc1 .Lar_EXITA_%=\n"
"s_add_u32 s80, s78, s91\n"
"s_addc_u32 s81, s79, 0\n"
"s_add_u32 s74, s66, 1\n"
"s_and_b32 s74, s74, 3\n"
"s_lshl_b32 s74, s74, 16\n"
"s_add_u32 s74, s74, 524288\n"
"s_add_u32 s82, s48, s74\n"
"s_addc_u32 s83, s49, 0\n"
"s_waitcnt vmcnt(2) lgkmcnt(0)\n"
"s_barrier\n"
"ds_read_b128 v[160:163], v235 offset:0\n"
"ds_read_b128 v[164:167], v235 offset:1024\n"
"s_add_u32 s66, s66, 1\n"
"s_add_u32 s52, s52, 16384\n"
"s_addc_u32 s53, s53, 0\n"
"s_and_b32 s74, s66, 3\n"
"s_waitcnt lgkmcnt(0)\n"
"v_smfmac_f32_16x16x64_bf16 v[200:203], v[160:163], v[128:135], v224\n"
"s_lshl2_add_u32 s74, s74, s71\n"
"s_add_u32 s74, s74, 32\n"
"s_lshl_b32 s74, s74, 14\n"
"v_smfmac_f32_16x16x64_bf16 v[200:203], v[164:167], v[96:103], v224\n"
"s_add_u32 s76, s48, s74\n"
"s_addc_u32 s77, s49, 0\n"
"s_andn2_b32 s74, 1, s66\n"
"v_smfmac_f32_16x16x64_bf16 v[204:207], v[160:163], v[136:143], v224\n"
"s_lshl_b32 s74, s74, 20\n"
"s_add_u32 s74, s74, s91\n"
"s_add_u32 s40, s48, s74\n"
"v_smfmac_f32_16x16x64_bf16 v[204:207], v[164:167], v[104:111], v224\n"
"s_addc_u32 s41, s49, 0\n"
"s_add_u32 s74, s66, 3\n"
"v_add_f32 v200, v200, v202\n"
"v_smfmac_f32_16x16x64_bf16 v[208:211], v[160:163], v[144:151], v224\n"
"s_and_b32 s74, s74, 3\n"
"s_lshl_b32 s74, s74, 16\n"
"v_add_f32 v201, v201, v203\n"
"v_smfmac_f32_16x16x64_bf16 v[208:211], v[164:167], v[112:119], v224\n"
"s_add_u32 s74, s74, 524288\n"
"s_add_u32 s84, s48, s74\n"
"v_add_f32 v204, v204, v206\n"
"v_smfmac_f32_16x16x64_bf16 v[212:215], v[160:163], v[152:159], v224\n"
"s_addc_u32 s85, s49, 0\n"
"s_nop 0\n"
"v_add_f32 v205, v205, v207\n"
"v_smfmac_f32_16x16x64_bf16 v[212:215], v[164:167], v[120:127], v224\n"
"v_cvt_pk_bf16_f32 v216, v200, v204\n"
"v_cvt_pk_bf16_f32 v218, v201, v205\n"
"v_add_f32 v208, v208, v210\n"
"v_add_f32 v209, v209, v211\n"
"s_nop 3\n"
"v_add_f32 v212, v212, v214\n"
"v_add_f32 v213, v213, v215\n"
"v_cvt_pk_bf16_f32 v217, v208, v212\n"
"v_cvt_pk_bf16_f32 v219, v209, v213\n"
"global_store_dwordx4 v238, v[216:219], s[78:79] sc1\n"
"s_memtime s[72:73]\n"
"s_waitcnt vmcnt(2)\n"
".Lar_EMT_%=:\n"
"v_subrev_u32 v254, s69, v250\n"
"v_and_b32 v255, 63, v254\n"
"v_cmp_ne_u32 vcc, 0, v255\n"
"v_max_i32_dpp v254, v254, v254 row_shr:1 row_mask:0xf bank_mask:0xf\n"
"s_cbranch_vccnz .Lar_ESLOW_%=\n"
".Lar_EOK_%=:\n"
"s_nop 0\n"
"v_max_i32_dpp v254, v254, v254 row_shr:2 row_mask:0xf bank_mask:0xf\n"
"s_and_b32 s74, s66, 1\n"
"s_lshl_b32 s74, s74, 20\n"
"v_max_i32_dpp v254, v254, v254 row_shr:4 row_mask:0xf bank_mask:0xf\n"
"s_add_u32 s78, s48, s74\n"
"s_addc_u32 s79, s49, 0\n"
"v_max_i32_dpp v254, v254, v254 row_shr:8 row_mask:0xf bank_mask:0xf\n"
"s_nop 1\n"
"v_max_i32_dpp v254, v254, v254 row_bcast:15 row_mask:0xa bank_mask:0xf\n"
"s_nop 1\n"
"v_max_i32_dpp v254, v254, v254 row_bcast:31 row_mask:0xc bank_mask:0xf\n"
"s_nop 0\n"
"v_readlane_b32 s39, v254, 63\n"
"s_nop 0\n"
"s_ashr_i32 s39, s39, 6\n"
"s_min_u32 s39, s39, 0x3000\n"
"s_add_i32 s39, s39, s38\n"
"s_add_i32 s38, s39, s61\n"
"s_max_i32 s38, s38, 0\n"
"s_mul_i32 s38, s38, s89\n"
"s_lshr_b32 s38, s38, 16\n"
"s_add_u32 s38, s38, s56\n"
"s_movk_i32 s75, 0x400\n"
".Lar_TSR_%=:\n"
".Lar_TWR_%=:\n"
"s_memtime s[42:43]\n"
"s_sub_u32 s75, s75, 1\n"
"s_cbranch_scc1 .Lar_TWER_%=\n"
"s_waitcnt lgkmcnt(0)\n"
"s_sub_i32 s74, s38, s42\n"
"s_cmp_gt_i32 s74, 400\n"
"s_cbranch_scc1 .Lar_TSR_%=\n"
"s_max_i32 s74, s74, 0\n"
".p2align 4\n"
".Lar_TDR_%=:\n"
"s_sub_u32 s74, s74, 28\n"
"s_cbranch_scc0 .Lar_TDR_%=\n"
".Lar_TWER_%=:\n"
"global_load_dwordx4 v[168:171], v225, s[80:81] offset:0 sc1\n"
"global_load_dwordx4 v[172:175], v225, s[80:81] offset:1024 sc1\n"
"global_load_dwordx4 v[176:179], v225, s[80:81] offset:2048 sc1\n"
"global_load_dwordx4 v[180:183], v225, s[80:81] offset:3072 sc1\n"
"ds_read_b128 v[188:191], v225 offset:31296\n"
"s_sub_u32 s90, s72, s57\n"
"s_min_u32 s90, s90, 2200\n"
"s_add_i32 s70, s39, s62\n"
"s_max_i32 s70, s70, 0\n"
"s_mul_i32 s70, s70, s89\n"
"s_lshr_b32 s70, s70, 16\n"
"s_add_u32 s70, s70, s56\n"
"v_mov_b32 v254, s70\n"
"v_mov_b32 v255, 31232\n"
"ds_write_b32 v255, v254\n"
"ds_read_b128 v[96:99], v236 offset:0\n"
"ds_read_b128 v[100:103], v236 offset:1024\n"
"ds_read_b128 v[104:107], v236 offset:2048\n"
"ds_read_b128 v[108:111], v236 offset:3072\n"
"ds_read_b128 v[112:115], v236 offset:4096\n"
"ds_read_b128 v[116:119], v236 offset:5120\n"
"ds_read_b128 v[120:123], v236 offset:6144\n"
"ds_read_b128 v[124:127], v236 offset:7168\n"
"v_mov_b64 v[200:201], 0\n"
"v_mov_b64 v[202:203], 0\n"
"v_mov_b64 v[204:205], 0\n"
"v_mov_b64 v[206:207], 0\n"
"v_mov_b64 v[208:209], 0\n"
"v_mov_b64 v[210:211], 0\n"
"v_mov_b64 v[212:213], 0\n"
"v_mov_b64 v[214:215], 0\n"
"v_mov_b64 v[216:217], 0\n"
"v_mov_b64 v[218:219], 0\n"
"v_mov_b64 v[220:221], 0\n"
"v_mov_b64 v[222:223], 0\n"
"s_waitcnt lgkmcnt(8)\n"
"s_barrier\n"
"s_waitcnt lgkmcnt(4)\n"
"ds_read_b128 v[128:131], v225 offset:8192\n"
"ds_read_b128 v[132:135], v225 offset:9216\n"
"ds_read_b128 v[136:139], v225 offset:10240\n"
"ds_read_b128 v[140:143], v225 offset:11264\n"
"ds_read_b128 v[144:147], v225 offset:12288\n"
"ds_read_b128 v[148:151], v225 offset:13312\n"
"ds_read_b128 v[152:155], v225 offset:14336\n"
"ds_read_b128 v[156:159], v225 offset:15360\n"
"ds_read_b128 v[192:195], v226 offset:16384\n"
"ds_read_b128 v[196:199], v226 offset:17408\n"
".Lar_RRE_%=:\n"
"s_waitcnt vmcnt(3)\n"
"v_mfma_f32_16x16x32_bf16 v[184:187], v[188:191], v[168:171], 0\n"
"s_waitcnt vmcnt(2)\n"
"v_mfma_f32_16x16x32_bf16 v[184:187], v[188:191], v[172:175], v[184:187]\n"
"s_waitcnt vmcnt(1)\n"
"v_mfma_f32_16x16x32_bf16 v[184:187], v[188:191], v[176:179], v[184:187]\n"
"s_waitcnt vmcnt(0)\n"
"v_mfma_f32_16x16x32_bf16 v[184:187], v[188:191], v[180:183], v[184:187]\n"
"s_nop 7\n"
"v_add_f32_dpp v184, v184, v184 row_ror:8 row_mask:0xf bank_mask:0xf\n"
"v_add_f32_dpp v185, v185, v185 row_ror:8 row_mask:0xf bank_mask:0xf\n"
"v_add_f32_dpp v186, v186, v186 row_ror:8 row_mask:0xf bank_mask:0xf\n"
"v_add_f32_dpp v187, v187, v187 row_ror:8 row_mask:0xf bank_mask:0xf\n"
"v_cmp_u_f32 vcc, v184, v184\n"
"v_cvt_pk_bf16_f32 v184, v184, v185\n"
"v_cvt_pk_bf16_f32 v185, v186, v187\n"
"s_cbranch_vccnz .Lar_RSLOW_%=\n"
".Lar_ROK_%=:\n"
"global_store_dwordx2 v239, v[184:185], s[82:83] sc1\n"
"s_mov_b64 exec, -1\n"
"s_waitcnt lgkmcnt(0)\n"
"s_movk_i32 s75, 0x400\n"
".Lar_TSA_%=:\n"
".Lar_TWA_%=:\n"
"s_memtime s[42:43]\n"
"s_sub_u32 s75, s75, 1\n"
"s_cbranch_scc1 .Lar_TWEA_%=\n"
"s_waitcnt lgkmcnt(0)\n"
"s_sub_i32 s74, s70, s42\n"
"s_cmp_gt_i32 s74, 400\n"
"s_cbranch_scc1 .Lar_TSA_%=\n"
"s_max_i32 s74, s74, 0\n"
".p2align 4\n"
".Lar_TDA_%=:\n"
"s_sub_u32 s74, s74, 28\n"
"s_cbranch_scc0 .Lar_TDA_%=\n"
".Lar_TWEA_%=:\n"
"global_load_dwordx4 v[160:163], v237, s[76:77] sc1\n"
"global_load_dwordx4 v[164:167], v237, s[76:77] offset:1024 sc1\n"
"s_memtime s[42:43]\n"
"s_memrealtime s[44:45]\n"
"s_nop 2\n"
"s_memrealtime s[46:47]\n"
"s_nop 2\n"
"s_memrealtime s[98:99]\n"
"s_nop 2\n"
"s_memrealtime s[96:97]\n"
"s_nop 2\n"
"s_memrealtime s[94:95]\n"
"s_nop 2\n"
"s_memrealtime s[92:93]\n"
"s_waitcnt lgkmcnt(0)\n"
"s_add_u32 s44, s44, s46\n"
"s_add_u32 s44, s44, s98\n"
"s_add_u32 s44, s44, s96\n"
"s_add_u32 s44, s44, s94\n"
"s_add_u32 s44, s44, s92\n"
"s_lshl_b32 s44, s44, 2\n"
"s_sub_u32 s74, s42, s56\n"
"s_sub_u32 s75, s44, s67\n"
"s_mov_b32 s56, s42\n"
"s_mov_b32 s67, s44\n"
"s_nop 7\n"
"s_nop 7\n"
"global_load_dwordx4 v[168:171], v237, s[76:77] sc1\n"
"global_load_dwordx4 v[172:175], v237, s[76:77] offset:1024 sc1\n"
"v_cvt_f32_u32 v254, s74\n"
"v_cvt_f32_u32 v255, s75\n"
"v_rcp_f32 v176, v254\n"
"v_rcp_f32 v177, v255\n"
"v_mul_f32 v254, 0x47800000, v254\n"
"v_mul_f32 v255, 0x47800000, v255\n"
"v_mul_f32 v176, v176, v255\n"
"v_mul_f32 v177, v177, v254\n"
"v_cvt_u32_f32 v176, v176\n"
"v_cvt_u32_f32 v177, v177\n"
"v_readfirstlane_b32 s88, v176\n"
"v_readfirstlane_b32 s89, v177\n"
"s_branch .Lar_LOOPA_%=\n"
".Lar_EXITA_%=:\n"
"s_waitcnt vmcnt(0)\n"
"s_barrier\n"
"v_mov_b32 v254, 64\n"
"v_mov_b32 v255, 0\n"
"s_mov_b64 exec, 1\n"
"global_store_dword v255, v254, s[54:55] sc1\n"
"s_mov_b64 exec, -1\n"
"s_branch .Lar_DONE_%=\n"
".Lar_MSLOWA_%=:\n"
"s_nop 6\n"
"v_mov_b64 v[200:201], 0\n"
"v_mov_b64 v[202:203], 0\n"
"v_mov_b64 v[204:205], 0\n"
"v_mov_b64 v[206:207], 0\n"
"v_mov_b64 v[208:209], 0\n"
"v_mov_b64 v[210:211], 0\n"
"v_mov_b64 v[212:213], 0\n"
"v_mov_b64 v[214:215], 0\n"
"s_waitcnt vmcnt(0) lgkmcnt(0)\n"
"v_cmp_u_f32 vcc, v168, v169\n"
"v_cmp_u_f32 s[72:73], v170, v171\n"
"v_cmp_u_f32 s[42:43], v172, v173\n"
"v_cmp_u_f32 s[74:75], v174, v175\n"
"s_or_b64 vcc, vcc, s[72:73]\n"
"s_or_b64 vcc, vcc, s[42:43]\n"
"s_or_b64 vcc, vcc, s[74:75]\n"
"s_cbranch_vccnz .Lar_MEXA_%=\n"
"v_mov_b64 v[160:161], v[168:169]\n"
"v_mov_b64 v[162:163], v[170:171]\n"
"v_mov_b64 v[164:165], v[172:173]\n"
"v_mov_b64 v[166:167], v[174:175]\n"
"s_branch .Lar_LOOPA_%=\n"
".Lar_MEXA_%=:\n"
"global_load_dwordx4 v[168:171], v237, s[76:77] sc1\n"
"global_load_dwordx4 v[172:175], v237, s[76:77] offset:1024 sc1\n"
"s_mov_b32 s74, 0x7f817f81\n"
"s_mov_b64 s[72:73], 0\n"
"v_cmp_eq_u32 vcc, s74, v160\n"
"s_or_b64 s[72:73], s[72:73], vcc\n"
"v_cmp_eq_u32 vcc, s74, v161\n"
"s_or_b64 s[72:73], s[72:73], vcc\n"
"v_cmp_eq_u32 vcc, s74, v162\n"
"s_or_b64 s[72:73], s[72:73], vcc\n"
"v_cmp_eq_u32 vcc, s74, v163\n"
"s_or_b64 s[72:73], s[72:73], vcc\n"
"v_cmp_eq_u32 vcc, s74, v164\n"
"s_or_b64 s[72:73], s[72:73], vcc\n"
"v_cmp_eq_u32 vcc, s74, v165\n"
"s_or_b64 s[72:73], s[72:73], vcc\n"
"v_cmp_eq_u32 vcc, s74, v166\n"
"s_or_b64 s[72:73], s[72:73], vcc\n"
"v_cmp_eq_u32 vcc, s74, v167\n"
"s_or_b64 s[72:73], s[72:73], vcc\n"
"s_cbranch_scc0 .Lar_MRDA_%=\n"
"s_sub_u32 s68, s68, 1\n"
"s_cbranch_scc1 .Lar_MTOA_%=\n"
"s_waitcnt vmcnt(0)\n"
"v_mov_b64 v[160:161], v[168:169]\n"
"v_mov_b64 v[162:163], v[170:171]\n"
"v_mov_b64 v[164:165], v[172:173]\n"
"v_mov_b64 v[166:167], v[174:175]\n"
"global_load_dwordx4 v[168:171], v237, s[76:77] sc1\n"
"global_load_dwordx4 v[172:175], v237, s[76:77] offset:1024 sc1\n"
"s_branch .Lar_LOOPA_%=\n"
".Lar_MTOA_%=:\n"
"s_mov_b32 s68, 0\n"
".Lar_MRDA_%=:\n"
"v_smfmac_f32_16x16x64_bf16 v[200:203], v[160:163], a[0:7], v224\n"
"v_smfmac_f32_16x16x64_bf16 v[204:207], v[160:163], a[64:71], v224\n"
"v_smfmac_f32_16x16x64_bf16 v[208:211], v[160:163], a[128:135], v224\n"
"v_smfmac_f32_16x16x64_bf16 v[212:215], v[160:163], a[192:199], v224\n"
"v_smfmac_f32_16x16x64_bf16 v[200:203], v[164:167], a[8:15], v224\n"
"s_branch .Lar_MOKA_%=\n"
".Lar_ESLOW_%=:\n"
"s_sub_u32 s68, s68, 1\n"
"s_cbranch_scc0 .Lar_ERE_%=\n"
"s_mov_b32 s68, 0\n"
"v_mov_b32 v254, 0\n"
"s_nop 3\n"
"s_branch .Lar_EOK_%=\n"
".Lar_ERE_%=:\n"
"v_lshlrev_b32 v255, 3, v225\n"
"global_load_dword v250, v255, s[86:87] sc1\n"
"s_waitcnt vmcnt(0)\n"
"s_branch .Lar_EMT_%=\n"
".Lar_RSLOW_%=:\n"
"s_mov_b32 s74, 0x7f817f81\n"
"v_cmp_eq_u32 vcc, s74, v168\n"
"s_cbranch_vccnz .Lar_RRL_%=\n"
"v_cmp_eq_u32 vcc, s74, v169\n"
"s_cbranch_vccnz .Lar_RRL_%=\n"
"v_cmp_eq_u32 vcc, s74, v170\n"
"s_cbranch_vccnz .Lar_RRL_%=\n"
"v_cmp_eq_u32 vcc, s74, v171\n"
"s_cbranch_vccnz .Lar_RRL_%=\n"
"v_cmp_eq_u32 vcc, s74, v172\n"
"s_cbranch_vccnz .Lar_RRL_%=\n"
"v_cmp_eq_u32 vcc, s74, v173\n"
"s_cbranch_vccnz .Lar_RRL_%=\n"
"v_cmp_eq_u32 vcc, s74, v174\n"
"s_cbranch_vccnz .Lar_RRL_%=\n"
"v_cmp_eq_u32 vcc, s74, v175\n"
"s_cbranch_vccnz .Lar_RRL_%=\n"
"v_cmp_eq_u32 vcc, s74, v176\n"
"s_cbranch_vccnz .Lar_RRL_%=\n"
"v_cmp_eq_u32 vcc, s74, v177\n"
"s_cbranch_vccnz .Lar_RRL_%=\n"
"v_cmp_eq_u32 vcc, s74, v178\n"
"s_cbranch_vccnz .Lar_RRL_%=\n"
"v_cmp_eq_u32 vcc, s74, v179\n"
"s_cbranch_vccnz .Lar_RRL_%=\n"
"v_cmp_eq_u32 vcc, s74, v180\n"
"s_cbranch_vccnz .Lar_RRL_%=\n"
"v_cmp_eq_u32 vcc, s74, v181\n"
"s_cbranch_vccnz .Lar_RRL_%=\n"
"v_cmp_eq_u32 vcc, s74, v182\n"
"s_cbranch_vccnz .Lar_RRL_%=\n"
"v_cmp_eq_u32 vcc, s74, v183\n"
"s_cbranch_vccnz .Lar_RRL_%=\n"
"s_branch .Lar_ROK_%=\n"
".Lar_RRL_%=:\n"
"s_sub_u32 s68, s68, 1\n"
"s_cbranch_scc1 .Lar_RTO_%=\n"
"global_load_dwordx4 v[168:171], v225, s[80:81] offset:0 sc1\n"
"global_load_dwordx4 v[172:175], v225, s[80:81] offset:1024 sc1\n"
"global_load_dwordx4 v[176:179], v225, s[80:81] offset:2048 sc1\n"
"global_load_dwordx4 v[180:183], v225, s[80:81] offset:3072 sc1\n"
"s_branch .Lar_RRE_%=\n"
".Lar_RTO_%=:\n"
"s_mov_b32 s68, 0\n"
"s_branch .Lar_ROK_%=\n"
".Lar_STARTB_%=:\n"
"s_cmp_eq_u32 s58, 3\n"
"s_cselect_b32 s46, 1, 0\n"
"s_mov_b32 s47, 0\n"
"s_sub_u32 s74, s58, 1\n"
"s_lshl_b32 s74, s74, 10\n"
"s_add_u32 s44, s74, 8192\n"
"v_add_u32 v250, s74, v225\n"
"s_lshl_b32 s74, s58, 12\n"
"s_and_b32 s75, s74, 0x2000\n"
"s_add_u32 s74, s74, s75\n"
"v_add_u32 v239, s74, v225\n"
"ds_read_b128 v[96:99], v236 offset:0\n"
"ds_read_b128 v[100:103], v236 offset:1024\n"
"ds_read_b128 v[104:107], v236 offset:2048\n"
"ds_read_b128 v[108:111], v236 offset:3072\n"
"ds_read_b128 v[112:115], v236 offset:4096\n"
"ds_read_b128 v[116:119], v236 offset:5120\n"
"ds_read_b128 v[120:123], v236 offset:6144\n"
"ds_read_b128 v[124:127], v236 offset:7168\n"
"v_mov_b64 v[200:201], 0\n"
"v_mov_b64 v[202:203], 0\n"
"v_mov_b64 v[204:205], 0\n"
"v_mov_b64 v[206:207], 0\n"
"v_mov_b64 v[208:209], 0\n"
"v_mov_b64 v[210:211], 0\n"
"v_mov_b64 v[212:213], 0\n"
"v_mov_b64 v[214:215], 0\n"
"v_mov_b64 v[216:217], 0\n"
"v_mov_b64 v[218:219], 0\n"
"v_mov_b64 v[220:221], 0\n"
"v_mov_b64 v[222:223], 0\n"
"s_and_b32 s74, s66, 3\n"
"s_lshl2_add_u32 s74, s74, s71\n"
"s_add_u32 s74, s74, 32\n"
"s_lshl_b32 s74, s74, 14\n"
"s_add_u32 s76, s48, s74\n"
"s_addc_u32 s77, s49, 0\n"
"s_andn2_b32 s74, 1, s66\n"
"s_lshl_b32 s74, s74, 20\n"
"s_add_u32 s74, s74, s91\n"
"s_lshl_b32 s75, s58, 10\n"
"s_add_u32 s74, s74, s75\n"
"s_add_u32 s40, s48, s74\n"
"s_addc_u32 s41, s49, 0\n"
"s_and_b32 s74, s66, 1\n"
"s_lshl_b32 s74, s74, 20\n"
"s_add_u32 s78, s48, s74\n"
"s_addc_u32 s79, s49, 0\n"
"global_load_dwordx4 v[160:163], v237, s[76:77] sc1\n"
"global_load_dwordx4 v[164:167], v237, s[76:77] offset:1024 sc1\n"
"s_add_u32 s74, s66, 0\n"
"s_min_u32 s74, s74, 63\n"
"s_cmp_lt_u32 s74, s67\n"
"s_cbranch_scc0 .Lar_RDYB0_%=\n"
".Lar_RDYBB0_%=:\n"
"global_load_dwordx4 v[168:171], v239, s[50:51] offset:0 sc1\n"
"global_load_dwordx4 v[172:175], v239, s[50:51] offset:1024 sc1\n"
"global_load_dwordx4 v[176:179], v239, s[50:51] offset:2048 sc1\n"
"global_load_dwordx4 v[180:183], v239, s[50:51] offset:3072 sc1\n"
"global_load_dwordx4 v[184:187], v250, s[50:51] offset:0 sc1\n"
"global_load_dwordx4 v[188:191], v250, s[50:51] offset:1024 sc1\n"
"s_lshl_b32 s74, s58, 12\n"
"v_add_u32 v255, s74, v225\n"
"s_waitcnt vmcnt(5)\n"
"ds_write_b128 v255, v[168:171] offset:8192\n"
"s_waitcnt vmcnt(4)\n"
"ds_write_b128 v255, v[172:175] offset:9216\n"
"s_waitcnt vmcnt(3)\n"
"ds_write_b128 v255, v[176:179] offset:10240\n"
"s_waitcnt vmcnt(2)\n"
"ds_write_b128 v255, v[180:183] offset:11264\n"
"s_waitcnt vmcnt(1)\n"
"ds_write_b128 v250, v[184:187] offset:8192\n"
"s_waitcnt vmcnt(0)\n"
"ds_write_b128 v250, v[188:191] offset:9216\n"
"s_add_u32 s50, s50, 32768\n"
"s_addc_u32 s51, s51, 0\n"
"s_add_u32 s74, s66, 1\n"
"s_min_u32 s74, s74, 63\n"
"s_cmp_lt_u32 s74, s67\n"
"s_cbranch_scc0 .Lar_RDYB1_%=\n"
".Lar_RDYBB1_%=:\n"
"v_lshlrev_b32 v254, 3, v225\n"
"v_and_b32 v255, 0x180, v254\n"
"v_and_b32 v254, 0x200, v254\n"
"v_lshl_add_u32 v255, v254, 4, v255\n"
"s_waitcnt vmcnt(0) lgkmcnt(0)\n"
"s_barrier\n"
"ds_read_b128 v[128:131], v225 offset:8192\n"
"ds_read_b128 v[132:135], v225 offset:9216\n"
"ds_read_b128 v[136:139], v225 offset:10240\n"
"ds_read_b128 v[140:143], v225 offset:11264\n"
"ds_read_b128 v[144:147], v225 offset:12288\n"
"ds_read_b128 v[148:151], v225 offset:13312\n"
"ds_read_b128 v[152:155], v225 offset:14336\n"
"ds_read_b128 v[156:159], v225 offset:15360\n"
"ds_read_b128 v[192:195], v226 offset:16384\n"
"ds_read_b128 v[196:199], v226 offset:17408\n"
"v_mov_b64 v[168:169], v[160:161]\n"
"v_mov_b64 v[170:171], v[162:163]\n"
"v_mov_b64 v[172:173], v[164:165]\n"
"v_mov_b64 v[174:175], v[166:167]\n"
"s_mov_b32 s36, 0x00ff00ff\n"
"s_mov_b32 s37, 0x00ff00ff\n"
".p2align 6\n"
".Lar_LOOPB_%=:\n"
"s_waitcnt vmcnt(3)\n"
"ds_write_b128 v226, v[160:163]\n"
"s_waitcnt vmcnt(2)\n"
"ds_write_b128 v226, v[164:167] offset:1024\n"
"v_smfmac_f32_16x16x64_bf16 v[200:203], v[160:163], a[0:7], v224\n"
"v_cmp_u_f32 vcc, v160, v161\n"
"v_smfmac_f32_16x16x64_bf16 v[204:207], v[160:163], a[64:71], v224\n"
"v_cmp_u_f32 s[72:73], v162, v163\n"
"v_smfmac_f32_16x16x64_bf16 v[208:211], v[160:163], a[128:135], v224\n"
"s_or_b64 vcc, vcc, s[72:73]\n"
"v_cmp_u_f32 s[42:43], v164, v165\n"
"v_smfmac_f32_16x16x64_bf16 v[212:215], v[160:163], a[192:199], v224\n"
"s_or_b64 vcc, vcc, s[42:43]\n"
"v_cmp_u_f32 s[74:75], v166, v167\n"
"v_smfmac_f32_16x16x64_bf16 v[200:203], v[164:167], a[8:15], v224\n"
"s_or_b64 vcc, vcc, s[74:75]\n"
"s_cbranch_vccnz .Lar_MSLOWB_%=\n"
".Lar_MOKB_%=:\n"
"v_smfmac_f32_16x16x64_bf16 v[216:219], v[160:163], v[96:103], v224\n"
"v_smfmac_f32_16x16x64_bf16 v[220:223], v[160:163], v[104:111], v224\n"
"s_waitcnt vmcnt(0) lgkmcnt(0)\n"
"s_barrier\n"
"ds_read_b128 v[168:171], v227 offset:0\n"
"ds_read_b128 v[172:175], v227 offset:1024\n"
"v_smfmac_f32_16x16x64_bf16 v[204:207], v[164:167], a[72:79], v224\n"
"ds_read_b128 v[176:179], v228 offset:0\n"
"ds_read_b128 v[180:183], v228 offset:1024\n"
"ds_read_b128 v[184:187], v229 offset:0\n"
"v_smfmac_f32_16x16x64_bf16 v[208:211], v[164:167], a[136:143], v224\n"
"ds_read_b128 v[188:191], v229 offset:1024\n"
"v_smfmac_f32_16x16x64_bf16 v[212:215], v[164:167], a[200:207], v224\n"
"v_smfmac_f32_16x16x64_bf16 v[216:219], v[164:167], v[112:119], v224\n"
"s_waitcnt lgkmcnt(5)\n"
"v_smfmac_f32_16x16x64_bf16 v[200:203], v[168:171], a[16:23], v224\n"
"s_waitcnt lgkmcnt(4)\n"
"v_mov_b32 v160, 0x7f817f81\n"
"v_smfmac_f32_16x16x64_bf16 v[200:203], v[172:175], a[24:31], v224\n"
"s_waitcnt lgkmcnt(3)\n"
"v_mov_b32 v161, 0x7f817f81\n"
"v_smfmac_f32_16x16x64_bf16 v[200:203], v[176:179], a[32:39], v224\n"
"s_waitcnt lgkmcnt(2)\n"
"v_mov_b32 v162, 0x7f817f81\n"
"v_smfmac_f32_16x16x64_bf16 v[200:203], v[180:183], a[40:47], v224\n"
"s_waitcnt lgkmcnt(1)\n"
"v_mov_b32 v163, 0x7f817f81\n"
"v_smfmac_f32_16x16x64_bf16 v[200:203], v[184:187], a[48:55], v224\n"
"global_store_dwordx4 v225, v[160:163], s[40:41] sc1\n"
"s_waitcnt lgkmcnt(0)\n"
"v_smfmac_f32_16x16x64_bf16 v[200:203], v[188:191], a[56:63], v224\n"
"v_smfmac_f32_16x16x64_bf16 v[204:207], v[168:171], a[80:87], v224\n"
"v_mov_b32 v98, 0\n"
"v_smfmac_f32_16x16x64_bf16 v[204:207], v[172:175], a[88:95], v224\n"
"v_mov_b32 v97, s66\n"
"v_smfmac_f32_16x16x64_bf16 v[204:207], v[176:179], a[96:103], v224\n"
"global_load_dword v255, v255, s[56:57] sc1\n"
"s_nop 0\n"
"v_smfmac_f32_16x16x64_bf16 v[204:207], v[180:183], a[104:111], v224\n"
"s_mov_b64 exec, s[46:47]\n"
"global_store_dword v98, v97, s[54:55] sc1\n"
"s_mov_b64 exec, -1\n"
"v_smfmac_f32_16x16x64_bf16 v[204:207], v[184:187], a[112:119], v224\n"
"s_mov_b32 m0, s90\n"
"v_add_f32 v200, v200, v202\n"
"v_smfmac_f32_16x16x64_bf16 v[204:207], v[188:191], a[120:127], v224\n"
"global_load_lds_dwordx4 v239, s[50:51] offset:0 sc1\n"
"s_nop 0\n"
"v_add_f32 v201, v201, v203\n"
"v_smfmac_f32_16x16x64_bf16 v[208:211], v[168:171], a[144:151], v224\n"
"global_load_lds_dwordx4 v239, s[50:51] offset:1024 sc1\n"
"s_nop 1\n"
"v_smfmac_f32_16x16x64_bf16 v[208:211], v[172:175], a[152:159], v224\n"
"global_load_lds_dwordx4 v239, s[50:51] offset:2048 sc1\n"
"v_add_f32 v204, v204, v206\n"
"v_smfmac_f32_16x16x64_bf16 v[208:211], v[176:179], a[160:167], v224\n"
"global_load_lds_dwordx4 v239, s[50:51] offset:3072 sc1\n"
"s_mov_b32 m0, s44\n"
"v_add_f32 v205, v205, v207\n"
"v_smfmac_f32_16x16x64_bf16 v[208:211], v[180:183], a[168:175], v224\n"
"global_load_lds_dwordx4 v250, s[50:51] offset:0 sc1\n"
"v_cvt_pk_bf16_f32 v200, v200, v204\n"
"v_smfmac_f32_16x16x64_bf16 v[208:211], v[184:187], a[176:183], v224\n"
"global_load_lds_dwordx4 v250, s[50:51] offset:1024 sc1\n"
"v_cvt_pk_bf16_f32 v201, v201, v205\n"
"v_smfmac_f32_16x16x64_bf16 v[208:211], v[188:191], a[184:191], v224\n"
"ds_write2_b32 v230, v200, v201 offset0:0 offset1:72\n"
"s_nop 1\n"
"v_smfmac_f32_16x16x64_bf16 v[212:215], v[168:171], a[208:215], v224\n"
"s_nop 0\n"
"s_nop 0\n"
"v_smfmac_f32_16x16x64_bf16 v[212:215], v[172:175], a[216:223], v224\n"
"s_nop 1\n"
"v_add_f32 v208, v208, v210\n"
"v_smfmac_f32_16x16x64_bf16 v[212:215], v[176:179], a[224:231], v224\n"
"v_add_f32 v209, v209, v211\n"
"v_smfmac_f32_16x16x64_bf16 v[212:215], v[180:183], a[232:239], v224\n"
"s_waitcnt lgkmcnt(0)\n"
"s_barrier\n"
"v_smfmac_f32_16x16x64_bf16 v[212:215], v[184:187], a[240:247], v224\n"
"ds_read_b128 v[96:99], v232 offset:0\n"
"ds_read_b128 v[100:103], v232 offset:64\n"
"ds_read_b128 v[104:107], v232 offset:128\n"
"v_smfmac_f32_16x16x64_bf16 v[212:215], v[188:191], a[248:255], v224\n"
"ds_read_b128 v[108:111], v232 offset:192\n"
"s_nop 1\n"
"v_smfmac_f32_16x16x64_bf16 v[220:223], v[164:167], v[120:127], v224\n"
"s_nop 3\n"
"v_add_f32 v212, v212, v214\n"
"v_add_f32 v213, v213, v215\n"
"v_cvt_pk_bf16_f32 v208, v208, v212\n"
"v_cvt_pk_bf16_f32 v209, v209, v213\n"
"ds_write2_b32 v231, v208, v209 offset0:0 offset1:72\n"
"s_waitcnt lgkmcnt(1)\n"
"v_mfma_f32_16x16x32_bf16 v[200:203], v[128:131], v[96:99], 0\n"
"v_mfma_f32_16x16x32_bf16 v[204:207], v[144:147], v[96:99], 0\n"
"v_mfma_f32_16x16x32_bf16 v[200:203], v[132:135], v[100:103], v[200:203]\n"
"v_mfma_f32_16x16x32_bf16 v[204:207], v[148:151], v[100:103], v[204:207]\n"
"s_waitcnt lgkmcnt(0)\n"
"s_barrier\n"
"ds_read_b128 v[112:115], v233 offset:0\n"
"ds_read_b128 v[116:119], v233 offset:64\n"
"v_mfma_f32_16x16x32_bf16 v[200:203], v[136:139], v[104:107], v[200:203]\n"
"ds_read_b128 v[120:123], v233 offset:128\n"
"ds_read_b128 v[124:127], v233 offset:192\n"
"v_mfma_f32_16x16x32_bf16 v[204:207], v[152:155], v[104:107], v[204:207]\n"
"v_mfma_f32_16x16x32_bf16 v[200:203], v[140:143], v[108:111], v[200:203]\n"
"v_mfma_f32_16x16x32_bf16 v[204:207], v[156:159], v[108:111], v[204:207]\n"
"s_waitcnt lgkmcnt(3)\n"
"v_mfma_f32_16x16x32_bf16 v[208:211], v[112:115], v[96:99], 0\n"
"ds_read_b128 v[128:131], v236 offset:8192\n"
"s_waitcnt lgkmcnt(3)\n"
"v_mfma_f32_16x16x32_bf16 v[208:211], v[116:119], v[100:103], v[208:211]\n"
"ds_read_b128 v[132:135], v236 offset:9216\n"
"s_waitcnt lgkmcnt(3)\n"
"v_mfma_f32_16x16x32_bf16 v[208:211], v[120:123], v[104:107], v[208:211]\n"
"ds_read_b128 v[136:139], v236 offset:10240\n"
"v_max3_f32 v212, v200, v201, v202\n"
"s_waitcnt lgkmcnt(3)\n"
"v_mfma_f32_16x16x32_bf16 v[208:211], v[124:127], v[108:111], v[208:211]\n"
"v_max3_f32 v212, v212, v203, v204\n"
"v_max3_f32 v212, v212, v205, v206\n"
"v_smfmac_f32_16x16x64_bf16 v[216:219], v[168:171], v[0:7], v224\n"
"ds_read_b128 v[140:143], v236 offset:11264\n"
"v_max_f32 v212, v212, v207\n"
"v_smfmac_f32_16x16x64_bf16 v[216:219], v[172:175], v[16:23], v224\n"
"ds_read_b128 v[144:147], v236 offset:12288\n"
"ds_read_b128 v[148:151], v236 offset:13312\n"
"v_max3_f32 v213, v212, v208, v209\n"
"v_max3_f32 v212, v212, v208, v209\n"
"v_smfmac_f32_16x16x64_bf16 v[216:219], v[176:179], v[32:39], v224\n"
"ds_read_b128 v[152:155], v236 offset:14336\n"
"v_permlane32_swap_b32 v212, v213\n"
"v_max_f32 v214, v212, v213\n"
"v_max_f32 v212, v212, v213\n"
"v_smfmac_f32_16x16x64_bf16 v[216:219], v[180:183], v[48:55], v224\n"
"ds_read_b128 v[156:159], v236 offset:15360\n"
"v_permlane16_swap_b32 v212, v214\n"
"v_max_f32 v212, v212, v214\n"
"v_mul_f32 v214, s63, v212\n"
"v_fma_f32 v200, v200, s63, -v214\n"
"v_smfmac_f32_16x16x64_bf16 v[216:219], v[184:187], v[64:71], v224\n"
"ds_read_b128 v[96:99], v236 offset:16384\n"
"v_exp_f32 v200, v200\n"
"v_fma_f32 v201, v201, s63, -v214\n"
"v_smfmac_f32_16x16x64_bf16 v[216:219], v[188:191], v[80:87], v224\n"
"ds_read_b128 v[100:103], v236 offset:17408\n"
"v_exp_f32 v201, v201\n"
"v_fma_f32 v202, v202, s63, -v214\n"
"v_smfmac_f32_16x16x64_bf16 v[220:223], v[168:171], v[8:15], v224\n"
"ds_read_b128 v[104:107], v236 offset:18432\n"
"v_exp_f32 v202, v202\n"
"v_fma_f32 v203, v203, s63, -v214\n"
"v_smfmac_f32_16x16x64_bf16 v[220:223], v[172:175], v[24:31], v224\n"
"ds_read_b128 v[108:111], v236 offset:19456\n"
"ds_read_b64 v[252:253], v251\n"
"v_exp_f32 v203, v203\n"
"v_fma_f32 v204, v204, s63, -v214\n"
"v_smfmac_f32_16x16x64_bf16 v[220:223], v[176:179], v[40:47], v224\n"
"ds_read_b128 v[112:115], v236 offset:20480\n"
"v_exp_f32 v204, v204\n"
"v_fma_f32 v205, v205, s63, -v214\n"
"v_smfmac_f32_16x16x64_bf16 v[220:223], v[180:183], v[56:63], v224\n"
"ds_read_b128 v[116:119], v236 offset:21504\n"
"v_exp_f32 v205, v205\n"
"v_fma_f32 v206, v206, s63, -v214\n"
"v_smfmac_f32_16x16x64_bf16 v[220:223], v[184:187], v[72:79], v224\n"
"ds_read_b128 v[120:123], v236 offset:22528\n"
"v_exp_f32 v206, v206\n"
"v_fma_f32 v207, v207, s63, -v214\n"
"v_smfmac_f32_16x16x64_bf16 v[220:223], v[188:191], v[88:95], v224\n"
"ds_read_b128 v[124:127], v236 offset:23552\n"
"v_exp_f32 v207, v207\n"
"v_fma_f32 v208, v208, s63, -v214\n"
"v_fma_f32 v209, v209, s63, -v214\n"
"v_cvt_pk_bf16_f32 v160, v200, v201\n"
"v_cvt_pk_bf16_f32 v161, v202, v203\n"
"v_cvt_pk_bf16_f32 v162, v204, v205\n"
"v_cvt_pk_bf16_f32 v163, v206, v207\n"
"v_exp_f32 v208, v208\n"
"v_exp_f32 v209, v209\n"
"v_mfma_f32_16x16x32_bf16 v[168:171], v[240:243], v[160:163], 0\n"
"v_cvt_pk_bf16_f32 v246, v208, v209\n"
"v_add_f32 v216, v216, v218\n"
"v_mfma_f32_16x16x32_bf16 v[172:175], v[192:195], v[160:163], 0\n"
"v_add_f32 v217, v217, v219\n"
"v_add_f32 v220, v220, v222\n"
"s_nop 0\n"
"v_mfma_f32_16x16x16_bf16 v[168:171], v[240:241], v[246:247], v[168:171]\n"
"v_add_f32 v221, v221, v223\n"
"v_cvt_pk_bf16_f32 v244, v216, v217\n"
"v_mfma_f32_16x16x32_bf16 v[176:179], v[196:199], v[160:163], 0\n"
"v_cvt_pk_bf16_f32 v245, v220, v221\n"
"v_cvt_pk_bf16_f32 v249, v208, v209\n"
"s_nop 0\n"
"v_mfma_f32_16x16x16_bf16 v[172:175], v[244:245], v[246:247], v[172:175]\n"
"s_nop 0\n"
"v_rcp_f32 v168, v168\n"
"v_mfma_f32_16x16x16_bf16 v[176:179], v[244:245], v[248:249], v[176:179]\n"
"s_waitcnt lgkmcnt(4)\n"
"v_cndmask_b32 v252, v252, v244, s[4:5]\n"
"v_cndmask_b32 v253, v253, v245, s[4:5]\n"
"v_lshrrev_b32 v254, 1, v225\n"
"s_mov_b64 exec, s[36:37]\n"
"v_pk_mul_f32 v[172:173], v[172:173], v[168:169] op_sel_hi:[1,0]\n"
"v_pk_mul_f32 v[174:175], v[174:175], v[168:169] op_sel_hi:[1,0]\n"
"s_nop 0\n"
"v_pk_mul_f32 v[176:177], v[176:177], v[168:169] op_sel_hi:[1,0]\n"
"v_pk_mul_f32 v[178:179], v[178:179], v[168:169] op_sel_hi:[1,0]\n"
"v_cvt_pk_bf16_f32 v164, v172, v173\n"
"v_cvt_pk_bf16_f32 v165, v174, v175\n"
"v_cvt_pk_bf16_f32 v166, v176, v177\n"
"v_cvt_pk_bf16_f32 v167, v178, v179\n"
"ds_write_b128 v234, v[164:167]\n"
"s_mov_b64 exec, -1\n"
"v_mfma_f32_32x32x8_bf16 v[200:215], v[246:247], v[248:249], 0\n"
"global_store_dwordx2 v254, v[252:253], s[52:53] sc1\n"
"s_cmp_eq_u32 s66, 63\n"
"s_cbranch_scc1 .Lar_EXITB_%=\n"
"s_waitcnt vmcnt(8) lgkmcnt(0)\n"
"s_barrier\n"
"ds_read_b128 v[160:163], v235 offset:0\n"
"ds_read_b128 v[164:167], v235 offset:1024\n"
"s_nop 3\n"
"s_waitcnt lgkmcnt(0)\n"
"v_smfmac_f32_16x16x64_bf16 v[200:203], v[160:163], v[128:135], v224\n"
"v_smfmac_f32_16x16x64_bf16 v[200:203], v[164:167], v[96:103], v224\n"
"s_nop 2\n"
"v_smfmac_f32_16x16x64_bf16 v[204:207], v[160:163], v[136:143], v224\n"
"s_nop 2\n"
"v_smfmac_f32_16x16x64_bf16 v[204:207], v[164:167], v[104:111], v224\n"
"s_nop 1\n"
"v_add_f32 v200, v200, v202\n"
"v_smfmac_f32_16x16x64_bf16 v[208:211], v[160:163], v[144:151], v224\n"
"s_nop 1\n"
"v_add_f32 v201, v201, v203\n"
"v_smfmac_f32_16x16x64_bf16 v[208:211], v[164:167], v[112:119], v224\n"
"s_nop 1\n"
"v_add_f32 v204, v204, v206\n"
"v_smfmac_f32_16x16x64_bf16 v[212:215], v[160:163], v[152:159], v224\n"
"s_nop 1\n"
"v_add_f32 v205, v205, v207\n"
"v_smfmac_f32_16x16x64_bf16 v[212:215], v[164:167], v[120:127], v224\n"
"v_cvt_pk_bf16_f32 v216, v200, v204\n"
"v_cvt_pk_bf16_f32 v218, v201, v205\n"
"v_add_f32 v208, v208, v210\n"
"v_add_f32 v209, v209, v211\n"
"s_nop 3\n"
"v_add_f32 v212, v212, v214\n"
"v_add_f32 v213, v213, v215\n"
"v_cvt_pk_bf16_f32 v217, v208, v212\n"
"v_cvt_pk_bf16_f32 v219, v209, v213\n"
"global_store_dwordx4 v238, v[216:219], s[78:79] sc1\n"
"ds_read_b128 v[96:99], v236 offset:0\n"
"ds_read_b128 v[100:103], v236 offset:1024\n"
"ds_read_b128 v[104:107], v236 offset:2048\n"
"ds_read_b128 v[108:111], v236 offset:3072\n"
"ds_read_b128 v[112:115], v236 offset:4096\n"
"ds_read_b128 v[116:119], v236 offset:5120\n"
"ds_read_b128 v[120:123], v236 offset:6144\n"
"ds_read_b128 v[124:127], v236 offset:7168\n"
"v_mov_b64 v[200:201], 0\n"
"v_mov_b64 v[202:203], 0\n"
"v_mov_b64 v[204:205], 0\n"
"v_mov_b64 v[206:207], 0\n"
"v_mov_b64 v[208:209], 0\n"
"v_mov_b64 v[210:211], 0\n"
"v_mov_b64 v[212:213], 0\n"
"v_mov_b64 v[214:215], 0\n"
"v_mov_b64 v[216:217], 0\n"
"v_mov_b64 v[218:219], 0\n"
"v_mov_b64 v[220:221], 0\n"
"v_mov_b64 v[222:223], 0\n"
"s_add_u32 s66, s66, 1\n"
"s_add_u32 s74, s66, 1\n"
"s_cmp_lt_u32 s74, 64\n"
"s_cselect_b32 s74, 0x8000, 0\n"
"s_add_u32 s50, s50, s74\n"
"s_addc_u32 s51, s51, 0\n"
"s_add_u32 s52, s52, 16384\n"
"s_addc_u32 s53, s53, 0\n"
"s_and_b32 s74, s66, 3\n"
"s_lshl2_add_u32 s74, s74, s71\n"
"s_add_u32 s74, s74, 32\n"
"s_lshl_b32 s74, s74, 14\n"
"s_add_u32 s76, s48, s74\n"
"s_addc_u32 s77, s49, 0\n"
"s_andn2_b32 s74, 1, s66\n"
"s_lshl_b32 s74, s74, 20\n"
"s_add_u32 s74, s74, s91\n"
"s_lshl_b32 s75, s58, 10\n"
"s_add_u32 s74, s74, s75\n"
"s_add_u32 s40, s48, s74\n"
"s_addc_u32 s41, s49, 0\n"
"s_and_b32 s74, s66, 1\n"
"s_lshl_b32 s74, s74, 20\n"
"s_add_u32 s78, s48, s74\n"
"s_addc_u32 s79, s49, 0\n"
"s_waitcnt vmcnt(2)\n"
"v_bfe_u32 v254, v225, 4, 2\n"
"v_lshl_add_u32 v255, v255, 2, v254\n"
"s_nop 1\n"
"v_min_u32_dpp v255, v255, v255 row_shr:1 row_mask:0xf bank_mask:0xf\n"
"s_nop 1\n"
"v_min_u32_dpp v255, v255, v255 row_shr:2 row_mask:0xf bank_mask:0xf\n"
"s_nop 1\n"
"v_min_u32_dpp v255, v255, v255 row_shr:4 row_mask:0xf bank_mask:0xf\n"
"s_nop 0\n"
"v_readlane_b32 s74, v255, 7\n"
"s_nop 0\n"
"s_max_u32 s67, s67, s74\n"
"s_barrier\n"
"ds_read_b128 v[128:131], v225 offset:8192\n"
"ds_read_b128 v[132:135], v225 offset:9216\n"
"ds_read_b128 v[136:139], v225 offset:10240\n"
"ds_read_b128 v[140:143], v225 offset:11264\n"
"ds_read_b128 v[144:147], v225 offset:12288\n"
"ds_read_b128 v[148:151], v225 offset:13312\n"
"ds_read_b128 v[152:155], v225 offset:14336\n"
"ds_read_b128 v[156:159], v225 offset:15360\n"
"ds_read_b128 v[192:195], v226 offset:16384\n"
"ds_read_b128 v[196:199], v226 offset:17408\n"
"v_mov_b32 v254, 31232\n"
"ds_read_b32 v254, v254\n"
"s_waitcnt lgkmcnt(0)\n"
"v_readfirstlane_b32 s70, v254\n"
"s_add_u32 s74, s66, 1\n"
"s_min_u32 s74, s74, 63\n"
"s_cmp_lt_u32 s74, s67\n"
"s_cbranch_scc0 .Lar_RDYB2_%=\n"
".Lar_RDYBB2_%=:\n"
"v_lshlrev_b32 v254, 3, v225\n"
"v_and_b32 v255, 0x180, v254\n"
"v_and_b32 v254, 0x200, v254\n"
"v_lshl_add_u32 v255, v254, 4, v255\n"
"s_movk_i32 s75, 0x400\n"
"s_branch .Lar_TWB_%=\n"
".Lar_TSB_%=:\n"
"s_sleep 2\n"
".Lar_TWB_%=:\n"
"s_memtime s[42:43]\n"
"s_sub_u32 s75, s75, 1\n"
"s_cbranch_scc1 .Lar_TWEB_%=\n"
"s_waitcnt lgkmcnt(0)\n"
"s_sub_i32 s74, s70, s42\n"
"s_cmp_gt_i32 s74, 540\n"
"s_cbranch_scc1 .Lar_TSB_%=\n"
"s_max_i32 s74, s74, 0\n"
".p2align 4\n"
".Lar_TDB_%=:\n"
"s_sub_u32 s74, s74, 28\n"
"s_cbranch_scc0 .Lar_TDB_%=\n"
".Lar_TWEB_%=:\n"
"global_load_dwordx4 v[160:163], v237, s[76:77] sc1\n"
"global_load_dwordx4 v[164:167], v237, s[76:77] offset:1024 sc1\n"
"s_nop 15\n"
"s_nop 15\n"
"s_nop 10\n"
"s_nop 7\n"
"s_nop 7\n"
"global_load_dwordx4 v[168:171], v237, s[76:77] sc1\n"
"global_load_dwordx4 v[172:175], v237, s[76:77] offset:1024 sc1\n"
"s_branch .Lar_LOOPB_%=\n"
".Lar_EXITB_%=:\n"
"s_waitcnt vmcnt(0)\n"
"s_barrier\n"
"s_branch .Lar_DONE_%=\n"
".Lar_RDYB0_%=:\n"
"s_nop 7\n"
"v_lshlrev_b32 v254, 3, v225\n"
"v_and_b32 v255, 0x180, v254\n"
"v_and_b32 v254, 0x200, v254\n"
"v_lshl_add_u32 v254, v254, 4, v255\n"
"global_load_dword v254, v254, s[56:57] sc1\n"
"v_lshrrev_b32 v255, 7, v255\n"
"s_waitcnt vmcnt(0)\n"
"v_lshl_add_u32 v254, v254, 2, v255\n"
"s_nop 1\n"
"v_min_u32_dpp v254, v254, v254 row_shr:1 row_mask:0xf bank_mask:0xf\n"
"s_nop 1\n"
"v_min_u32_dpp v254, v254, v254 row_shr:2 row_mask:0xf bank_mask:0xf\n"
"s_nop 1\n"
"v_min_u32_dpp v254, v254, v254 row_shr:4 row_mask:0xf bank_mask:0xf\n"
"s_nop 0\n"
"v_readlane_b32 s67, v254, 7\n"
"s_nop 3\n"
"s_cmp_lt_u32 s74, s67\n"
"s_cbranch_scc1 .Lar_RDYDB0_%=\n"
"s_sleep 1\n"
"s_sub_u32 s68, s68, 1\n"
"s_cbranch_scc0 .Lar_RDYB0_%=\n"
"s_mov_b32 s68, 0\n"
"s_mov_b32 s67, 64\n"
".Lar_RDYDB0_%=:\n"
"s_branch .Lar_RDYBB0_%=\n"
".Lar_RDYB1_%=:\n"
"s_nop 7\n"
"v_lshlrev_b32 v254, 3, v225\n"
"v_and_b32 v255, 0x180, v254\n"
"v_and_b32 v254, 0x200, v254\n"
"v_lshl_add_u32 v254, v254, 4, v255\n"
"global_load_dword v254, v254, s[56:57] sc1\n"
"v_lshrrev_b32 v255, 7, v255\n"
"s_waitcnt vmcnt(0)\n"
"v_lshl_add_u32 v254, v254, 2, v255\n"
"s_nop 1\n"
"v_min_u32_dpp v254, v254, v254 row_shr:1 row_mask:0xf bank_mask:0xf\n"
"s_nop 1\n"
"v_min_u32_dpp v254, v254, v254 row_shr:2 row_mask:0xf bank_mask:0xf\n"
"s_nop 1\n"
"v_min_u32_dpp v254, v254, v254 row_shr:4 row_mask:0xf bank_mask:0xf\n"
"s_nop 0\n"
"v_readlane_b32 s67, v254, 7\n"
"s_nop 3\n"
"s_cmp_lt_u32 s74, s67\n"
"s_cbranch_scc1 .Lar_RDYDB1_%=\n"
"s_sleep 1\n"
"s_sub_u32 s68, s68, 1\n"
"s_cbranch_scc0 .Lar_RDYB1_%=\n"
"s_mov_b32 s68, 0\n"
"s_mov_b32 s67, 64\n"
".Lar_RDYDB1_%=:\n"
"s_branch .Lar_RDYBB1_%=\n"
".Lar_MSLOWB_%=:\n"
"s_nop 6\n"
"v_mov_b64 v[200:201], 0\n"
"v_mov_b64 v[202:203], 0\n"
"v_mov_b64 v[204:205], 0\n"
"v_mov_b64 v[206:207], 0\n"
"v_mov_b64 v[208:209], 0\n"
"v_mov_b64 v[210:211], 0\n"
"v_mov_b64 v[212:213], 0\n"
"v_mov_b64 v[214:215], 0\n"
"s_waitcnt vmcnt(0) lgkmcnt(0)\n"
"v_cmp_u_f32 vcc, v168, v169\n"
"v_cmp_u_f32 s[72:73], v170, v171\n"
"v_cmp_u_f32 s[42:43], v172, v173\n"
"v_cmp_u_f32 s[74:75], v174, v175\n"
"s_or_b64 vcc, vcc, s[72:73]\n"
"s_or_b64 vcc, vcc, s[42:43]\n"
"s_or_b64 vcc, vcc, s[74:75]\n"
"s_cbranch_vccnz .Lar_MEXB_%=\n"
"v_mov_b64 v[160:161], v[168:169]\n"
"v_mov_b64 v[162:163], v[170:171]\n"
"v_mov_b64 v[164:165], v[172:173]\n"
"v_mov_b64 v[166:167], v[174:175]\n"
"s_branch .Lar_LOOPB_%=\n"
".Lar_MEXB_%=:\n"
"global_load_dwordx4 v[168:171], v237, s[76:77] sc1\n"
"global_load_dwordx4 v[172:175], v237, s[76:77] offset:1024 sc1\n"
"s_mov_b32 s74, 0x7f817f81\n"
"s_mov_b64 s[72:73], 0\n"
"v_cmp_eq_u32 vcc, s74, v160\n"
"s_or_b64 s[72:73], s[72:73], vcc\n"
"v_cmp_eq_u32 vcc, s74, v161\n"
"s_or_b64 s[72:73], s[72:73], vcc\n"
"v_cmp_eq_u32 vcc, s74, v162\n"
"s_or_b64 s[72:73], s[72:73], vcc\n"
"v_cmp_eq_u32 vcc, s74, v163\n"
"s_or_b64 s[72:73], s[72:73], vcc\n"
"v_cmp_eq_u32 vcc, s74, v164\n"
"s_or_b64 s[72:73], s[72:73], vcc\n"
"v_cmp_eq_u32 vcc, s74, v165\n"
"s_or_b64 s[72:73], s[72:73], vcc\n"
"v_cmp_eq_u32 vcc, s74, v166\n"
"s_or_b64 s[72:73], s[72:73], vcc\n"
"v_cmp_eq_u32 vcc, s74, v167\n"
"s_or_b64 s[72:73], s[72:73], vcc\n"
"s_cbranch_scc0 .Lar_MRDB_%=\n"
"s_sub_u32 s68, s68, 1\n"
"s_cbranch_scc1 .Lar_MTOB_%=\n"
"s_waitcnt vmcnt(0)\n"
"v_mov_b64 v[160:161], v[168:169]\n"
"v_mov_b64 v[162:163], v[170:171]\n"
"v_mov_b64 v[164:165], v[172:173]\n"
"v_mov_b64 v[166:167], v[174:175]\n"
"global_load_dwordx4 v[168:171], v237, s[76:77] sc1\n"
"global_load_dwordx4 v[172:175], v237, s[76:77] offset:1024 sc1\n"
"s_branch .Lar_LOOPB_%=\n"
".Lar_MTOB_%=:\n"
"s_mov_b32 s68, 0\n"
".Lar_MRDB_%=:\n"
"v_smfmac_f32_16x16x64_bf16 v[200:203], v[160:163], a[0:7], v224\n"
"v_smfmac_f32_16x16x64_bf16 v[204:207], v[160:163], a[64:71], v224\n"
"v_smfmac_f32_16x16x64_bf16 v[208:211], v[160:163], a[128:135], v224\n"
"v_smfmac_f32_16x16x64_bf16 v[212:215], v[160:163], a[192:199], v224\n"
"v_smfmac_f32_16x16x64_bf16 v[200:203], v[164:167], a[8:15], v224\n"
"s_branch .Lar_MOKB_%=\n"
".Lar_RDYB2_%=:\n"
"s_nop 7\n"
"v_lshlrev_b32 v254, 3, v225\n"
"v_and_b32 v255, 0x180, v254\n"
"v_and_b32 v254, 0x200, v254\n"
"v_lshl_add_u32 v254, v254, 4, v255\n"
"global_load_dword v254, v254, s[56:57] sc1\n"
"v_lshrrev_b32 v255, 7, v255\n"
"s_waitcnt vmcnt(0)\n"
"v_lshl_add_u32 v254, v254, 2, v255\n"
"s_nop 1\n"
"v_min_u32_dpp v254, v254, v254 row_shr:1 row_mask:0xf bank_mask:0xf\n"
"s_nop 1\n"
"v_min_u32_dpp v254, v254, v254 row_shr:2 row_mask:0xf bank_mask:0xf\n"
"s_nop 1\n"
"v_min_u32_dpp v254, v254, v254 row_shr:4 row_mask:0xf bank_mask:0xf\n"
"s_nop 0\n"
"v_readlane_b32 s67, v254, 7\n"
"s_nop 3\n"
"s_cmp_lt_u32 s74, s67\n"
"s_cbranch_scc1 .Lar_RDYDB2_%=\n"
"s_sleep 1\n"
"s_sub_u32 s68, s68, 1\n"
"s_cbranch_scc0 .Lar_RDYB2_%=\n"
"s_mov_b32 s68, 0\n"
"s_mov_b32 s67, 64\n"
".Lar_RDYDB2_%=:\n"
"s_branch .Lar_RDYBB2_%=\n"
".Lar_DONE_%=:\n"
"s_waitcnt vmcnt(0) lgkmcnt(0)\n"
    : "+{v[224:239]}"(va), "+{v[240:247]}"(vb), "+{v251}"(vkp), "+{v[200:215]}"(sa), "+{v[216:223]}"(sb), "+{v[252:253]}"(sc), "+{v254}"(xi), "+{v255}"(ro)
    :
    : "memory", "vcc", "scc", V10(), V10(1), V10(2), V10(3), V10(4), V10(5), V10(6), V10(7), V10(8), V10(9), V10(10), V10(11), V10(12), V10(13), V10(14), V10(15), V10(16), V10(17), V10(18), V10(19),
      "v248", "v249", "v250",
      A10(), A10(1), A10(2), A10(3), A10(4), A10(5), A10(6), A10(7), A10(8), A10(9), A10(10), A10(11), A10(12), A10(13), A10(14), A10(15), A10(16), A10(17), A10(18), A10(19), A10(20), A10(21), A10(22), A10(23), A10(24),
      "a250", "a251", "a252", "a253", "a254", "a255",

      "s4", "s5", "s36", "s37", "s38", "s39", S10(4), S10(5), S10(6), S10(7), S10(8), S10(9));
}

#define MF_WX(acc, w, x) asm volatile("v_mfma_f32_16x16x32_bf16 %0, %1, %2, %0" : "+v"(acc) : "a"(w), "v"(x))
#define MF_WX0(acc, w, x) asm volatile("v_mfma_f32_16x16x32_bf16 %0, %1, %2, 0" : "=&v"(acc) : "a"(w), "v"(x))
#define MF_XW(acc, x, w) asm volatile("v_mfma_f32_16x16x32_bf16 %0, %1, %2, %0" : "+v"(acc) : "v"(x), "a"(w))
#define MF_XW0(acc, x, w) asm volatile("v_mfma_f32_16x16x32_bf16 %0, %1, %2, 0" : "=&v"(acc) : "v"(x), "a"(w))
#define MF_VV(acc, a, b) asm volatile("s_nop 1\n\tv_mfma_f32_16x16x32_bf16 %0, %1, %2, %0" : "+v"(acc) : "v"(a), "v"(b))
#define MF_VV0(acc, a, b) asm volatile("s_nop 1\n\tv_mfma_f32_16x16x32_bf16 %0, %1, %2, 0" : "=&v"(acc) : "v"(a), "v"(b))
#define MF16_VV(acc, a, b) asm volatile("s_nop 1\n\tv_mfma_f32_16x16x16_bf16 %0, %1, %2, %0" : "+v"(acc) : "v"(a), "v"(b))
#define MF16_0(acc, a, b) asm volatile("v_mfma_f32_16x16x16_bf16 %0, %1, %2, 0" : "=&v"(acc) : "v"(a), "v"(b))
#define MF16(acc, a, b) asm volatile("v_mfma_f32_16x16x16_bf16 %0, %1, %2, %0" : "+v"(acc) : "v"(a), "v"(b))
#define MF_FENCE4(a, b, c, d) asm volatile("s_nop 7" : "+v"(a), "+v"(b), "+v"(c), "+v"(d))
#define VM_WAIT(n) asm volatile("s_waitcnt vmcnt(" #n ")" ::: "memory")
#define BAR() asm volatile("s_waitcnt lgkmcnt(0)\n\ts_barrier" ::: "memory")

DEV void dma16(unsigned voff, const char* base, unsigned lds) {
  asm volatile("s_mov_b32 m0, %2\n\ts_nop 0\n\tglobal_load_lds_dwordx4 %0, %1" ::"v"(voff), "s"(base), "s"(lds) : "memory");
}
DEV void st_sc1(void* g, v4u v) { asm volatile("global_store_dwordx4 %0, %1, off sc1\n\ts_nop 1" ::"v"(g), "v"(v) : "memory"); }
DEV void tr_chunk(unsigned a0, unsigned a1, unsigned a2, unsigned a3, v4u& f0, v4u& f1, v4u& f2, v4u& f3) {
  v2u t0, t1, t2, t3, t4, t5, t6, t7;
  asm volatile(
      "ds_read_b64_tr_b16 %0, %8\n\tds_read_b64_tr_b16 %1, %8 offset:2048\n\tds_read_b64_tr_b16 %2, %9\n\tds_read_b64_tr_b16 %3, %9 offset:2048\n\t"
      "ds_read_b64_tr_b16 %4, %10\n\tds_read_b64_tr_b16 %5, %10 offset:2048\n\tds_read_b64_tr_b16 %6, %11\n\tds_read_b64_tr_b16 %7, %11 offset:2048\n\ts_waitcnt lgkmcnt(0)"
      : "=&a"(t0), "=&a"(t1), "=&a"(t2), "=&a"(t3), "=&a"(t4), "=&a"(t5), "=&a"(t6), "=&a"(t7)
      : "v"(a0), "v"(a1), "v"(a2), "v"(a3) : "memory");
  f0 = (v4u){t0.x, t0.y, t1.x, t1.y}; f1 = (v4u){t2.x, t2.y, t3.x, t3.y}; f2 = (v4u){t4.x, t4.y, t5.x, t5.y}; f3 = (v4u){t6.x, t6.y, t7.x, t7.y};
  asm volatile("" : "+a"(f0), "+a"(f1), "+a"(f2), "+a"(f3));
}
#define PST(k)
#define PST2(k)
#define SST(st, sg)
#define SDUMP()
#define PT(k)
#define PDUMP()
typedef __amdgpu_buffer_rsrc_t rsrc_t;
DEV v4u bld(rsrc_t r, unsigned v, unsigned s) { return __builtin_amdgcn_raw_buffer_load_b128(r, v, s, 16); }
DEV v2u bld8(rsrc_t r, unsigned v, unsigned s) { return __builtin_amdgcn_raw_buffer_load_b64(r, v, s, 16); }
DEV void bst(rsrc_t r, unsigned v, unsigned s, v4u d) { __builtin_amdgcn_raw_buffer_store_b128(d, r, v + s, 0, 16); }
#define FR(k) l16 + ((k) & 3) * 1024
#define FS(b, k) (b) + ((k) >> 3) * 16384 + (((k) >> 2) & 1) * 4096
#define OPQ_S(x) asm volatile("" : "+s"(x))
#define OPQ_V(x) asm volatile("" : "+v"(x))
DEV v4u ldsr(unsigned a) { return *(LAS v4u*)a; }
DEV v4f ldsf(unsigned a) { return *(LAS v4f*)a; }

struct TrAddr { unsigned a[4]; };
DEV TrAddr tr_addr(unsigned base, int l) {
  const int rho = l >> 2;
  TrAddr t;
#pragma unroll
  for (int i = 0; i < 4; ++i) t.a[i] = base + rho * 128 + ((i ^ ((rho >> 1) & 3)) * 32) + (l & 3) * 8;
  return t;
}
struct Stg { int g0, q, tile, half; };
DEV Stg stg(int l) { Stg s; s.g0 = l >> 5; s.q = (l >> 3) & 3; s.tile = ((l >> 1) & 3) ^ ((l >> 4) & 3); s.half = l & 1; return s; }

template <int SLEEP = 2>
DEV void wait_ge(const char* a, unsigned target, int& budget) {
  if (threadIdx.x < 64) {
    for (;;) {
      const unsigned v = __hip_atomic_load((const GAS unsigned*)a, __ATOMIC_RELAXED, __HIP_MEMORY_SCOPE_AGENT);
      if (__all(v >= target)) break;
      if (--budget < 0) break;
      __builtin_amdgcn_s_sleep(SLEEP);
    }
  }
  asm volatile("s_barrier" ::: "memory");
}
DEV void signal(char* a, unsigned v) {
  asm volatile("s_waitcnt vmcnt(0)\n\ts_barrier" ::: "memory");
  if (threadIdx.x == 0) __hip_atomic_store((GAS unsigned*)a, v, __ATOMIC_RELAXED, __HIP_MEMORY_SCOPE_AGENT);
}
DEV const char* uni(const char* p) {
  return (const char*)(((size_t)(unsigned)__builtin_amdgcn_readfirstlane(hi32(p)) << 32) | (unsigned)__builtin_amdgcn_readfirstlane(lo32(p)));
}
#define XCLOB "v191", "v192","v193","v194","v195","v196","v197","v198","v199", V10(20), V10(21), V10(22), V10(23), V10(24), "v250","v251","v252","v253","v254","v255"
template <int K, int OFF>
DEV void ldx(unsigned voff, const char* base) {
  asm volatile("global_load_dwordx4 v[%c2:%c3], %0, %1 offset:%c4 sc1" ::"v"(voff), "s"(base), "i"(192 + 4 * K), "i"(195 + 4 * K), "i"(OFF) : "memory", XCLOB);
}
template <int K, int OFF>
DEV void wrx(unsigned a) { asm volatile("ds_write_b128 %0, v[%c1:%c2] offset:%c3" ::"v"(a), "i"(192 + 4 * K), "i"(195 + 4 * K), "i"(OFF) : "memory"); }
template <int K> DEV void ldf(unsigned l16, const char* b) { ldx<K, (K & 3) * 1024>(l16, b + (K >> 3) * 16384 + ((K >> 2) & 1) * 4096); }
template <int N, int I = 0> DEV void ldf_all(unsigned l16, const char* b) { if constexpr (I < N) { ldf<I>(l16, b); ldf_all<N, I + 1>(l16, b); } }
template <int STR, int I = 0> DEV void wrx_all(unsigned a) { if constexpr (I < 8) { wrx<I, I * STR>(a); wrx_all<STR, I + 1>(a); } }
template <int K, bool FIRST>
DEV void mf_x(v4f& acc, const v4u& w) {
  if (FIRST) asm volatile("v_mfma_f32_16x16x32_bf16 %0, %1, v[%c2:%c3], 0" : "=&v"(acc) : "a"(w), "i"(192 + 4 * K), "i"(195 + 4 * K));
  else asm volatile("v_mfma_f32_16x16x32_bf16 %0, %1, v[%c2:%c3], %0" : "+v"(acc) : "a"(w), "i"(192 + 4 * K), "i"(195 + 4 * K));
}
DEV void h1_ld(unsigned l16, const char* h1, int k) {
  if (k == 0) ldf<0>(l16, h1); if (k == 1) ldf<1>(l16, h1); if (k == 2) ldf<2>(l16, h1); if (k == 3) ldf<3>(l16, h1);
  if (k == 4) ldf<4>(l16, h1); if (k == 5) ldf<5>(l16, h1); if (k == 6) ldf<6>(l16, h1); if (k == 7) ldf<7>(l16, h1);
}
DEV void h1_wr(unsigned a, int k) {
  if (k == 0) wrx<0, 0>(a); if (k == 1) wrx<1, 1024>(a); if (k == 2) wrx<2, 2048>(a); if (k == 3) wrx<3, 3072>(a);
  if (k == 4) wrx<4, 4096>(a); if (k == 5) wrx<5, 5120>(a); if (k == 6) wrx<6, 6144>(a); if (k == 7) wrx<7, 7168>(a);
}
template <int OFF>
DEV void st_s(unsigned voff, const char* base, v4u v) {
  asm volatile("global_store_dwordx4 %0, %1, %2 offset:%3 sc1\n\ts_nop 1" ::"v"(voff), "v"(v), "s"(base), "i"(OFF) : "memory");
}
template <bool ISV>
DEV void qkv_store(unsigned l16, const char* d, const v4u (&o)[4], int k) {
  if (k == 0) st_s<0>(l16, d, o[0]);
  if (k == 1) st_s<1024>(l16, d, o[1]);
  if (k == 2) { if (ISV) st_s<2048>(l16, d, o[2]); else st_s<0>(l16, d + 4096, o[2]); }
  if (k == 3) { if (ISV) st_s<3072>(l16, d, o[3]); else st_s<1024>(l16, d + 4096, o[3]); }
}
template <bool ISV>
DEV void qkv_loop(const Params& p, int pi, const v4u (&W)[64], int w, int l, const char* h1a, const char* h1b, const char* dst, unsigned dst_it, char* prog, int n, const char* pace, const char* lflag, char* lmine, const char* wa, const char* wb, int& budget) {
  const unsigned l16 = l * 16;
  v4u x[4][2], o[4];
  const int half = n >> 1; const unsigned hs = 4194304u / n;
  BAR();
  x[0][0] = ldsr(l16); x[0][1] = ldsr(l16 + 16384); x[1][0] = ldsr(l16 + 1024); x[1][1] = ldsr(l16 + 16384 + 1024);
  unsigned cur = 0, nxt = 32768, fre = 65536;
  for (int i = 0; i < n; ++i) {
    const unsigned xa = cur + l16, xn = nxt + l16, lw = xn + w * 8192;
    const int i2 = i + 2 < n ? i + 2 : i;
    const char* src = (i2 < half ? h1a : h1b) + (i2 & (half - 1)) * hs;
    const bool gate = CFG_QGATE && pace && 2 * i > CFG_QLEAD + 2, held = pace && 2 * i > CFG_QLEAD + 4;
    if (!CFG_QGATE && pace && 2 * i > CFG_QLEAD) wait_ge<16>(pace, 2 * i - CFG_QLEAD, budget);
    int ii = __builtin_amdgcn_readfirstlane(i); OPQ_S(ii);
    const char* d = dst + (ii - 1) * (long)dst_it;
    v4f acc[2][4];
#pragma unroll
    for (int ks = 0; ks < 16; ++ks) {
      if (!ISV && (ks == 0 || (ks == 8 && CFG_QGATE == 1)) && gate) {
        wait_ge<CFG_QPOLL>(pace, 2 * i - CFG_QLEAD + (CFG_QGATE == 1 ? ks >> 3 : pi & 1), budget);
        if (ks == 0) {
          if (held) ldf_all<8>(l16, (i + 1 < half ? h1a : h1b) + ((i + 1 < n ? i + 1 : i) & (half - 1)) * hs);
          qkv_store<ISV>(l16, d, o, 0); qkv_store<ISV>(l16, d, o, 1); qkv_store<ISV>(l16, d, o, 2); qkv_store<ISV>(l16, d, o, 3);
        }
        __builtin_amdgcn_s_sleep(CFG_QDEL);
      }
#pragma unroll
      for (int T = 0; T < 2; ++T) {
#pragma unroll
        for (int t = 0; t < 4; ++t) {
          if (ks == 0) { if (ISV) MF_XW0(acc[T][t], x[0][T], W[t]); else MF_WX0(acc[T][t], W[t], x[0][T]); }
          else { if (ISV) MF_XW(acc[T][t], x[ks & 3][T], W[ks * 4 + t]); else MF_WX(acc[T][t], W[ks * 4 + t], x[ks & 3][T]); }
          if (t == 0) {
            __builtin_amdgcn_sched_barrier(0);
            x[(ks + 2) & 3][T] = ks + 2 < 16 ? ldsr(xa + T * 16384 + (ks + 2) * 1024) : ldsr(xn + T * 16384 + (ks - 14) * 1024);
            __builtin_amdgcn_sched_barrier(0);
          }
          if (t == 2) {
            if (ks < 2 && i > 1 && !gate) qkv_store<ISV>(l16, d, o, ks * 2 + T);
            if (ks == 6 && T == 0) { if (i < 2) VM_WAIT(0); else VM_WAIT(4); }
            if (ks >= 6 && ks < 10) h1_wr(lw, (ks - 6) * 2 + T);
            if (ks == 11 && T == 1) {
              VM_WAIT(0);
              BAR();
              if (i == 0 && lmine && threadIdx.x == 0) asm volatile("global_store_dword %0, %1, off sc1" ::"v"(lmine), "v"(1) : "memory");
              if (i > 1 && threadIdx.x == 0) asm volatile("global_store_dword %0, %1, off sc1" ::"v"(prog), "v"(i) : "memory");
            }
            if (ks == 12 && T == 0 && wa && i && i < 15) {
              asm volatile("global_load_dwordx4 v[224:227], %0, %1\n\tglobal_load_dwordx4 v[228:231], %0, %2" ::"v"(l16), "s"(wa + (ii - 1) * 131072), "s"(wb + (ii - 1) * 131072) : "memory", XCLOB);
            }
            if (ks >= 12 && i && !gate) h1_ld(l16, src, (ks - 12) * 2 + T);
          }
        }
      }
    }
    MF_FENCE4(acc[1][0], acc[1][1], acc[1][2], acc[1][3]);
    asm volatile("" : "+v"(acc[0][0]), "+v"(acc[0][1]), "+v"(acc[0][2]), "+v"(acc[0][3]));
#pragma unroll
    for (int k = 0; k < 4; ++k) {
      if (ISV) o[k] = (v4u){pk2(acc[0][k][0], acc[0][k][1]), pk2(acc[0][k][2], acc[0][k][3]), pk2(acc[1][k][0], acc[1][k][1]), pk2(acc[1][k][2], acc[1][k][3])};
      else { const int T = k >> 1, u = k & 1;
        o[k] = (v4u){pk2(acc[T][2 * u][0], acc[T][2 * u + 1][0]), pk2(acc[T][2 * u][1], acc[T][2 * u + 1][1]), pk2(acc[T][2 * u][2], acc[T][2 * u + 1][2]), pk2(acc[T][2 * u][3], acc[T][2 * u + 1][3])}; }
    }
    if (i == 0 || i == n - 1) {
      const char* d0 = d + dst_it;
#pragma unroll
      for (int k = 0; k < 4; ++k) qkv_store<ISV>(l16, d0, o, k);
      if (i == 0) PST2(12);
      signal(prog, i + 1);
      if (i == 0) {
        PST(6);
        wait_ge(lflag, 1, budget);
        ldf_all<8>(l16, h1a + hs); VM_WAIT(0); wrx_all<1024>(lw); BAR();
        ldf_all<8>(l16, src);
        x[0][0] = ldsr(xn); x[0][1] = ldsr(xn + 16384); x[1][0] = ldsr(xn + 1024); x[1][1] = ldsr(xn + 16384 + 1024);
      }
    }
    const unsigned t = cur; cur = nxt; nxt = fre; fre = t;
  }
}

DEV void qkv_phase(const Params& p, int pi, int kind, int cbl, int phase, int w, int l, int& budget) {
  const u16* Wm = kind == 0 ? p.Wq : (kind == 1 ? p.Wk : p.Wv);
  const Stg s = stg(l);
  const unsigned* par = (const unsigned*)(p.ws + WS_CTL + CTL_PAR);
  const int hseg = (pi - 64) & 127;
  const int head = 2 * cbl + (w >> 1);
  if (kind == 0) wait_ge<32>(p.ws + WS_COMM + (comm_vote(p.ws) & 1) * 8192 + COMM_PROGA + 2 * cbl * 128, 1, budget);
  if (kind) for (int z = 0; z < phase * CFG_STAG; ++z) __builtin_amdgcn_s_sleep(8);
  v4u n0[8], n1[4], tt[8], sh[8]; unsigned pv;
  {
    const char* md = p.ws + WS_CTL + CTL_MOD + (w & 1) * 512;
    const char* na = p.ws + WS_N1 + phase * 32768 + w * 8192;
    const char* nb = p.ws + WS_N1 + hseg * 16384 + ((w & 1) * 8 + (w >> 1) * 4) * 1024;
    const unsigned mo = (l >> 4) * 16, po = (l < 8 ? 112 + l : (l == 8 ? RG_H1 : (l == 9 ? RG_H1 + 1 : (kind == 0 ? RG_Q + cbl : RG_KV + head)))) * 4;
#define LDP(d, v, b, o) asm volatile("global_load_dwordx4 %0, %1, %2 offset:%3" : "=&v"(d) : "v"(v), "s"(b), "i"(o) : "memory")
    asm volatile("global_load_dword %0, %1, %2" : "=&v"(pv) : "v"(po), "s"(par) : "memory");
#define LD3(k) LDP(n0[k], l * 16, na + (k >> 2) * 4096, (k & 3) * 1024); LDP(sh[k], mo, md, k * 64); LDP(tt[k], mo, md, 1024 + k * 64)
    LD3(0); LD3(1); LD3(2); LD3(3); LD3(4); LD3(5); LD3(6); LD3(7);
    LDP(n1[0], l * 16, nb, 0); LDP(n1[1], l * 16, nb, 1024); LDP(n1[2], l * 16, nb, 2048); LDP(n1[3], l * 16, nb, 3072);
  }
  const unsigned so = (8 * s.g0 + s.q) * 8192 + s.tile * 32 + s.half * 16;
  const char* src = (const char*)Wm + cbl * 512 + w * 128;
  const unsigned ring = 32768 + w * 32768;
#pragma unroll
  for (int c = 0; c < 8; ++c)
#pragma unroll
    for (int i = 0; i < 4; ++i) dma16(so, src + (c * 32 + 16 * (i & 1) + 4 * (i >> 1)) * 8192, ring + c * 4096 + i * 1024);
  asm volatile("s_waitcnt vmcnt(32)" : "+v"(pv), "+v"(n1[0]), "+v"(n1[1]), "+v"(n1[2]), "+v"(n1[3])::"memory");
#pragma unroll
  for (int k = 0; k < 8; ++k) asm volatile("" : "+v"(n0[k]), "+v"(tt[k]), "+v"(sh[k]));
  const char* h1a = p.ws + (size_t)RG_H1 * REG + (unsigned)__builtin_amdgcn_readlane(pv, 8);
  const char* h1b = p.ws + (size_t)(RG_H1 + 1) * REG + (unsigned)__builtin_amdgcn_readlane(pv, 9);
  const char* dst = (kind == 0 ? p.ws + (size_t)(RG_Q + cbl) * REG + (head & 1) * 1048576 + phase * 16384 + (w & 1) * 2048
                               : p.ws + (size_t)(RG_KV + head) * REG + phase * 32768 + (kind == 1 ? (w & 1) * 2048 : 16384 + (w & 1) * 4096)) + (unsigned)__builtin_amdgcn_readlane(pv, 10);
  const char* pace = p.ws + WS_COMM + (vote(pv) & 1) * 8192 + COMM_PROGA + 2 * cbl * 128;
  asm volatile("s_nop 4" : "+s"(h1a), "+s"(h1b), "+s"(dst), "+s"(pace));
#pragma unroll
  for (int k = 0; k < 8; ++k) *(LAS v4u*)(w * 8192 + k * 1024 + l * 16) = ln_mod(n0[k], tt[k], sh[k]);
  const TrAddr ta = tr_addr(ring, l);
  PST2(3);
  v4u W[64];
#pragma unroll
  for (int c = 0; c < 8; ++c) {
    const unsigned o = c * 4096;
    VM_WAIT(28);
    tr_chunk(ta.a[0] + o, ta.a[1] + o, ta.a[2] + o, ta.a[3] + o, W[c * 4], W[c * 4 + 1], W[c * 4 + 2], W[c * 4 + 3]);
    if (kind == 0) __builtin_amdgcn_s_sleep(16);
#pragma unroll
    for (int i = 0; i < 4; ++i) dma16(so, src + ((c + 8) * 32 + 16 * (i & 1) + 4 * (i >> 1)) * 8192, ring + c * 4096 + i * 1024);
  }
  __builtin_amdgcn_sched_barrier(0);
  if (w >> 1) { n1[0] = ln_mod(n1[0], tt[4], sh[4]); n1[1] = ln_mod(n1[1], tt[5], sh[5]); n1[2] = ln_mod(n1[2], tt[6], sh[6]); n1[3] = ln_mod(n1[3], tt[7], sh[7]); }
  else { n1[0] = ln_mod(n1[0], tt[0], sh[0]); n1[1] = ln_mod(n1[1], tt[1], sh[1]); n1[2] = ln_mod(n1[2], tt[2], sh[2]); n1[3] = ln_mod(n1[3], tt[3], sh[3]); }
  PST2(4);
  VM_WAIT(0);
  if (kind) {
    const char* d = (hseg < 64 ? h1a : h1b) + (hseg & 63) * 32768 + (w & 1) * 16384 + (w >> 1) * 4096;
    st_s<0>(l * 16, d, n1[0]); st_s<1024>(l * 16, d, n1[1]); st_s<2048>(l * 16, d, n1[2]); st_s<3072>(l * 16, d, n1[3]);
  }
#pragma unroll
  for (int c = 8; c < 16; ++c) {
    const unsigned o = (c & 7) * 4096;
    tr_chunk(ta.a[0] + o, ta.a[1] + o, ta.a[2] + o, ta.a[3] + o, W[c * 4], W[c * 4 + 1], W[c * 4 + 2], W[c * 4 + 3]);
  }
  const int xs = pi >> 5;
  const char* wa = CFG_TRK && xs >= 3 ? (const char*)(xs == 3 ? p.Wo : (xs == 4 ? p.wg : p.wd)) + ((pi & 31) * 4 + w) * 1024 : nullptr;
  const char* wb = xs == 3 ? wa + 2097152 : (xs == 4 ? (const char*)p.wu + ((pi & 31) * 4 + w) * 1024 : wa);
  char* lmine = kind ? p.ws + WS_CTL + CTL_PROGL + hseg * 128 : nullptr;
  const char* lflag = p.ws + WS_CTL + CTL_PROGL + (kind ? (((l >> 1) & 15) * 4 + phase) * 2 + (l & 1) : ((l >> 1) * 2 + phase) * 2 + (l & 1)) * 128;
  h1a += phase * 65536 + w * 16384; h1b += phase * 65536 + w * 16384;
  PST(0);
  char* prog = p.ws + WS_CTL + CTL_PROGP + ((kind * 16 + cbl) * 4 + phase) * 128;
  if (kind == 2) qkv_loop<true>(p, pi, W, w, l, h1a, h1b, dst, 131072, prog, 16, nullptr, lflag, lmine, wa, wb, budget);
  else qkv_loop<false>(p, pi, W, w, l, h1a, h1b, dst, kind == 1 ? 131072 : 32768, prog, kind == 1 ? 16 : 32, kind == 1 ? nullptr : pace, lflag, lmine, wa, wb, budget);
}

#define PARW(i) (unsigned)__builtin_amdgcn_readfirstlane(par[i])
template <int N> DEV void vm_wait() { asm volatile("s_waitcnt vmcnt(%0)" ::"i"(N) : "memory"); }
DEV unsigned wave_min(unsigned v) {
  v = min(v, (unsigned)__builtin_amdgcn_update_dpp((int)v, (int)v, 0x111, 0xf, 0xf, false));
  v = min(v, (unsigned)__builtin_amdgcn_update_dpp((int)v, (int)v, 0x112, 0xf, 0xf, false));
  v = min(v, (unsigned)__builtin_amdgcn_update_dpp((int)v, (int)v, 0x114, 0xf, 0xf, false));
  v = min(v, (unsigned)__builtin_amdgcn_update_dpp((int)v, (int)v, 0x118, 0xf, 0xf, false));
  v = min(v, (unsigned)__builtin_amdgcn_update_dpp((int)v, (int)v, 0x142, 0xa, 0xf, false));
  v = min(v, (unsigned)__builtin_amdgcn_update_dpp((int)v, (int)v, 0x143, 0xc, 0xf, false));
  return (unsigned)__builtin_amdgcn_readlane((int)v, 63);
}
template <int SLEEP, bool MIN = true>
DEV int poll_wave(const char* a, int sh, int add, int need, int& budget) {
  for (;;) {
    const unsigned v = (__hip_atomic_load((const GAS unsigned*)a, __ATOMIC_RELAXED, __HIP_MEMORY_SCOPE_AGENT) << sh) + add;
    if (__all(v >= (unsigned)need)) return MIN ? (int)wave_min(v) : need;
    if (--budget < 0) return 64;
    if (SLEEP) __builtin_amdgcn_s_sleep(SLEEP);
  }
}
DEV int poll_wg(const char* a, int sh, int add, int need, int& budget) {
  if (threadIdx.x < 64) poll_wave<CFG_SPOLL, false>(a, sh, add, need, budget);
  asm volatile("s_barrier" ::: "memory");
  return need;
}
DEV void flag_st(char* a, int v) { if (threadIdx.x == 0) asm volatile("global_store_dword %0, %1, off sc1" ::"v"(a), "v"(v) : "memory"); }
DEV void wave_signal(char* a) {
  VM_WAIT(0);
  if ((threadIdx.x & 63) == 0) __hip_atomic_fetch_add((GAS unsigned*)a, 1u, __ATOMIC_RELAXED, __HIP_MEMORY_SCOPE_AGENT);
}
template <bool PF, int N, int EXTRA, int K = 0>
DEV void roll_body(v4f (&a)[4], const v4u (&W)[4 * N], unsigned xl, const char* nx, int pend, char* myfl, int sg, const char* pf) {
  if constexpr (K < N) {
    vm_wait<(PF ? N - 1 : N - 1 - K) + EXTRA>();
    mf_x<K, K == 0>(a[0], W[K * 4]); mf_x<K, K == 0>(a[1], W[K * 4 + 1]); mf_x<K, K == 0>(a[2], W[K * 4 + 2]); mf_x<K, K == 0>(a[3], W[K * 4 + 3]);
    if (K == 0 && pend == 1) { asm volatile("s_barrier" ::: "memory"); flag_st(myfl, sg); }
    if (K == 6 && pend == 2) { vm_wait<(PF ? 6 : 0) + EXTRA>(); asm volatile("s_barrier" ::: "memory"); flag_st(myfl, sg); }
    if (K == 7 && threadIdx.x < 64) asm volatile("global_load_dword v191, %0, off sc1" ::"v"(pf) : "memory", XCLOB);
    if (PF) ldf<K>(xl, nx);
    roll_body<PF, N, EXTRA, K + 1>(a, W, xl, nx, pend, myfl, sg, pf);
  } else {
    MF_FENCE4(a[0], a[1], a[2], a[3]);
    vm_wait<PF ? N - 7 : 0>();
  }
}
template <int N, int EXTRA, bool FLAG, class Src, class Pre, class Mid, class Fin>
DEV void stream(const v4u (&W)[4 * N], unsigned xl, const char* pf, int psh, int padd, char* myfl, int& budget, Src src, Pre pre, Mid mid, Fin fin) {
  unsigned slot = 0;
  int nr = poll_wg(pf, psh, padd, 1, budget), pend = 0;
  ldf_all<N>(xl, src(0));
  for (int sg = 0; sg < 64; ++sg) {
    const char* nx = src(sg + 1 < 64 ? sg + 1 : sg);
    const bool pf_now = nr > sg + 1 && sg + 1 < 64;
    pre(sg);
    v4f a[4];
    if (pf_now) roll_body<true, N, EXTRA>(a, W, xl, nx, pend, myfl, sg, pf); else roll_body<false, N, EXTRA>(a, W, xl, nx, pend, myfl, sg, pf);
    slot ^= 4;
    if (threadIdx.x < 64) {
      unsigned v;
      asm volatile("v_mov_b32 %0, v191" : "=v"(v));
      *(volatile LAS int*)(163832 - slot) = (int)wave_min((v << psh) + padd);
    }
    mid(a, sg);
    BAR();
    nr = __builtin_amdgcn_readfirstlane(*(volatile LAS int*)(163832 - slot));
    fin(a, sg);
    if (pf_now) pend = FLAG ? 2 : 0;
    else if (nr > sg + 1 && sg + 1 < 64) { SST(0, sg + 1); ldf_all<N>(xl, nx); pend = FLAG ? 1 : 0; }
    else {
      if (FLAG) signal(myfl, sg + 1);
      pend = 0;
      if (sg + 1 < 64) { nr = poll_wg(pf, psh, padd, sg + 2, budget); SST(0, sg + 1); ldf_all<N>(xl, nx); }
    }
  }
}
template <int N, class F>
DEV void load_w(v4u (&W)[N * 4], int w, int l, unsigned so, F src) {
  constexpr int D = CFG_WDEPTH;
  const unsigned ring = w * 40960;
  const TrAddr ta = tr_addr(ring, l);
#pragma unroll
  for (int c = 0; c < N + D; ++c) {
    if (c >= D) {
      const int d = c - D, left = (c < N ? D : N - d) - 1;
      asm volatile("s_waitcnt vmcnt(%0)" ::"i"(4 * left) : "memory");
      const unsigned o = (d % D) * 4096;
      tr_chunk(ta.a[0] + o, ta.a[1] + o, ta.a[2] + o, ta.a[3] + o, W[d * 4], W[d * 4 + 1], W[d * 4 + 2], W[d * 4 + 3]);
    }
    if (c < N) {
#pragma unroll
      for (int i = 0; i < 4; ++i) dma16(so, src(c, i), ring + (c % D) * 4096 + i * 1024);
    }
  }
}
struct Dn {
  char* ws; const unsigned* par; char* fl; unsigned long long mask;
  DEV unsigned off(int rg) const { return rg * REG + ((unsigned)(mask >> (rg - RG_O)) & 1u) * 8192u; }
  DEV const char* at(int rg, unsigned o) const { return ws + off(rg) + o; }
};
constexpr int FL_T = 0, FL_R = 32768, FL_W = 49152, FL_G = 53248;

DEV void stage_t(const Params& p, const Dn& d, int q, int w, int l, int& budget) {
  const unsigned* par = d.par;
  const rsrc_t ws = __builtin_amdgcn_make_buffer_rsrc(p.ws, 0, 0x7fffffff, 0x00020000);
  const int g = l >> 4, c = l & 15, T = w & 1;
  const unsigned l16 = l * 16;
  const unsigned oKV = (RG_KV + q) * REG + PARW(RG_KV + q), oQ = (RG_Q + (q >> 1)) * REG + PARW(RG_Q + (q >> 1)) + (q & 1) * 1048576 + T * 4096;
  const unsigned oKM = (RG_KM + (q >> 1)) * REG + PARW(RG_KM + (q >> 1)) + (q & 1) * 4096;
  const char* pA = p.ws + WS_COMM + (comm_vote(p.ws) & 1) * 8192 + COMM_PROGA + ((l & 1) * 32 + q) * 128;
  const char* pP = p.ws + WS_CTL + CTL_PROGP + ((l & 8) ? (q >> 1) * 512 + (l & 1) * 128 : ((1 + ((l >> 2) & 1)) * 16 + (q >> 1)) * 512 + (l & 3) * 128);
  const int psh = (l & 8) ? 1 : 2, padd = (l & 8) ? (l & 1) : (l & 3);
  char* myfl = d.fl + hm(FL_T / 2 + (q * 4 + w) * 128);
  const float c1 = 0.08838834764831845f * 1.4426950408889634f;
  int nP = 0, nA = 0;
  v4u qf[4], kx[8], vx[8];
  auto fetch = [&](int seg) {
    if (nP <= seg) nP = poll_wave<8>(pP, psh, padd, seg + 1, budget);
#pragma unroll
    for (int u = 0; u < 4; ++u) qf[u] = bld(ws, FR(u), oQ + seg * 16384);
#pragma unroll
    for (int u = 0; u < 8; ++u) kx[u] = bld(ws, FR(u), FS(oKV + seg * 32768, u));
#pragma unroll
    for (int u = 0; u < 8; ++u) vx[u] = bld(ws, FR(u), FS(oKV + seg * 32768, 8 + u));
  };
  fetch(w >> 1);
  for (int seg = w >> 1; seg < 64; seg += 2) {
    const unsigned oO = d.off(RG_O + (seg >> 2)) + (seg & 3) * 524288 + T * 262144 + hm(q * 4096);
    v4f S0, S1, S2;
    MF_VV0(S1, kx[0], qf[0]); MF_VV0(S2, kx[4], qf[0]);
#pragma unroll
    for (int u = 1; u < 4; ++u) { MF_VV(S1, kx[u], qf[u]); MF_VV(S2, kx[4 + u], qf[u]); }
    asm volatile("s_nop 7" : "+v"(S1), "+v"(S2));
    if (nA <= seg) { if (seg < 58) nA = poll_wave<CFG_TPOLL>(pA, 0, 0, seg + 1, budget); else nA = poll_wave<1, false>(pA, 0, 0, seg + 1, budget); }
    asm volatile("" ::: "memory");
    SST(0, seg);
    const unsigned mo = oKM + seg * 16384;
    v4u km[4]; v2u vm[4];
#pragma unroll
    for (int u = 0; u < 4; ++u) km[u] = bld(ws, (2 * (c >> 2) + (c & 1)) * 256 + u * 64 + g * 16, mo);
#pragma unroll
    for (int u = 0; u < 4; ++u) vm[u] = bld8(ws, 2048 + u * 512 + l * 8, mo);
    const int tok = 16 * T + c;
    float e[10];
#pragma unroll
    for (int r = 0; r < 4; ++r) { e[r] = (4 * g + r <= tok) ? S1[r] : -INFINITY; e[4 + r] = (16 + 4 * g + r <= tok) ? S2[r] : -INFINITY; }
    float m = e[0];
#pragma unroll
    for (int i = 1; i < 8; ++i) m = fmaxf(m, e[i]);
    MF_VV0(S0, km[0], qf[0]);
#pragma unroll
    for (int u = 1; u < 4; ++u) MF_VV(S0, km[u], qf[u]);
    asm volatile("s_nop 7" : "+v"(S0));
    e[8] = S0[0]; e[9] = S0[1];
    m = fmaxf(m, fmaxf(e[8], e[9]));
    { const v2u t = __builtin_amdgcn_permlane32_swap(__float_as_uint(m), __float_as_uint(m), false, false); m = fmaxf(__uint_as_float(t[0]), __uint_as_float(t[1])); }
    { const v2u t = __builtin_amdgcn_permlane16_swap(__float_as_uint(m), __float_as_uint(m), false, false); m = fmaxf(__uint_as_float(t[0]), __uint_as_float(t[1])); }
    const float nbm = m * c1;
#pragma unroll
    for (int i = 0; i < 10; ++i) e[i] = __builtin_amdgcn_exp2f(__builtin_fmaf(e[i], c1, -nbm));
    const v4u px = {pk2(e[0], e[1]), pk2(e[2], e[3]), pk2(e[4], e[5]), pk2(e[6], e[7])};
    const unsigned pm = pk2(e[8], e[9]);
    const v2u pm0 = {pm, 0u}, pm1 = {0u, pm};
    const v4u ones = {0x3f803f80u, 0x3f803f80u, 0x3f803f80u, 0x3f803f80u};
    const v2u ones2 = {0x3f803f80u, 0x3f803f80u};
    v4f Z, O[8];
    MF_VV0(Z, ones, px);
#pragma unroll
    for (int t = 0; t < 8; ++t) { MF_VV0(O[t], vx[t], px); }
    MF16_VV(Z, ones2, pm0);
#pragma unroll
    for (int t = 0; t < 8; ++t) { if (t & 1) MF16_VV(O[t], vm[t >> 1], pm1); else MF16_VV(O[t], vm[t >> 1], pm0); }
    asm volatile("s_nop 7" : "+v"(Z), "+v"(O[0]), "+v"(O[1]), "+v"(O[2]), "+v"(O[3]), "+v"(O[4]), "+v"(O[5]), "+v"(O[6]), "+v"(O[7]));
    const float rz = __builtin_amdgcn_rcpf(Z[0]);
#pragma unroll
    for (int u = 0; u < 4; ++u)
      bst(ws, FR(u), oO, (v4u){pk2(O[2 * u][0] * rz, O[2 * u][1] * rz), pk2(O[2 * u][2] * rz, O[2 * u][3] * rz), pk2(O[2 * u + 1][0] * rz, O[2 * u + 1][1] * rz), pk2(O[2 * u + 1][2] * rz, O[2 * u + 1][3] * rz)});
    SST(1, seg);
    asm volatile("" ::: "memory");
    if (seg + 2 < 64 && nP > seg + 2 && nA > seg + 2) {
      fetch(seg + 2);
      VM_WAIT(20);
      if (l == 0) __hip_atomic_fetch_add((GAS unsigned*)myfl, 1u, __ATOMIC_RELAXED, __HIP_MEMORY_SCOPE_AGENT);
    } else {
      wave_signal(myfl);
      fetch(seg + 2 < 64 ? seg + 2 : seg);
    }
  }
}

DEV void stage_w(const Params& p, const Dn& d, int q, int w, int l, int& budget) {
  const Stg s = stg(l);
  const char* so = (const char*)p.Wo + ((q >> 3) * 1024 + (w >> 1) * 512) * 1024 + (q & 7) * 128;
  v4u W[64];
  load_w<16>(W, w, l, (4 * s.g0 + s.q) * 1024 + s.tile * 32 + s.half * 16, [&](int c, int i) { return so + (c * 32 + 8 * (i & 1) + 16 * (i >> 1)) * 1024; });
  const int pi = 96 + q; PST(2);
  const unsigned xo = (w & 1) * 262144 + ((q >> 3) * 4 + (w >> 1) * 2) * 16384;
  const int tok = w * 8 + (l >> 3);
  stream<16, 0, true>(W, l * 16, d.fl + hm(FL_T / 2 + ((q & 24) * 4 + (l & 31)) * 128), 1, (l >> 1) & 1, d.fl + FL_W + q * 128, budget,
    [&](int sg) { return d.at(RG_O + (sg >> 2), (sg & 3) * 524288 + xo); }, [&](int sg) {},
    [&](v4f (&a)[4], int sg) {
#pragma unroll
      for (int t = 0; t < 4; ++t) *(LAS v4f*)((sg & 1) * 17408 + (w >> 1) * 8704 + ((w & 1) * 16 + (l & 15)) * 272 + t * 64 + (l >> 4) * 16) = a[t];
    },
    [&](v4f (&a)[4], int sg) {
      const unsigned ra = (sg & 1) * 17408 + tok * 272 + (l & 7) * 32;
      const v4f s0 = ldsf(ra) + ldsf(ra + 8704), s1 = ldsf(ra + 16) + ldsf(ra + 8704 + 16);
      st_s<0>(hm(tok * 4096) + (l & 7) * 16, d.at(RG_PW + (sg >> 3), (sg & 7) * 262144 + q * 128), (v4u){pk2(s0[0], s0[1]), pk2(s0[2], s0[3]), pk2(s1[0], s1[1]), pk2(s1[2], s1[3])});
      SST(3, sg);
    });
}

DEV void stage_r(const Params& p, const Dn& d, int q, int w, int l, int& budget) {
  const rsrc_t ws = __builtin_amdgcn_make_buffer_rsrc(p.ws, 0, 0x7fffffff, 0x00020000);
  const unsigned l16 = l * 16;
  const char* md = p.ws + WS_CTL + CTL_MOD + l16;
  const v4u ga = *(const v4u*)(md + 2048), sh = *(const v4u*)(md + 3072), tt = *(const v4u*)(md + 4096);
  const char* pf = d.fl + FL_W + (l & 31) * 128;
  char* myfl = d.fl + FL_R + q * 128;
  const int row = (q & 7) * 4 + w;
  int nr = 0, cnt = 0;
  for (int sg = q >> 3; sg < 64; sg += 2) {
    const v4u xv = *(const v4u*)(p.x + (sg * 32 + row) * 512 + l * 8);
    const unsigned oP = d.off(RG_PW + (sg >> 3)) + (sg & 7) * 262144 + hm(row * 4096);
    const unsigned oH = d.off(RG_H2 + (sg >> 5)) + (sg & 31) * 65536 + ((q >> 2) & 1) * 32768 + hm(w * 4096) + (q & 3) * 256;
    const unsigned oX = d.off(RG_X2 + (sg >> 5)) + (sg & 31) * 65536 + hm(row * 1024);
    if (sg >= 40) poll_wg(pf, 0, 0, sg + 1, budget); else if (nr <= sg) nr = poll_wave<2>(pf, 0, 0, sg + 1, budget);
    asm volatile("" ::: "memory");
    SST(4, sg);
    v4u pv[4];
#pragma unroll
    for (int k = 0; k < 4; ++k) pv[k] = bld(ws, FR(k), oP);
    float v[8]; v4u x2;
#pragma unroll
    for (int j = 0; j < 4; ++j) {
      const unsigned at = pk2((blo(pv[0][j]) + blo(pv[1][j])) + (blo(pv[2][j]) + blo(pv[3][j])), (bhi(pv[0][j]) + bhi(pv[1][j])) + (bhi(pv[2][j]) + bhi(pv[3][j])));
      const unsigned gt = pk2(blo(ga[j]) * blo(at), bhi(ga[j]) * bhi(at));
      x2[j] = pk2(blo(xv[j]) + blo(gt), bhi(xv[j]) + bhi(gt));
      v[2 * j] = blo(x2[j]); v[2 * j + 1] = bhi(x2[j]);
    }
    bst(ws, l16, oX, x2);
    *(LAS v4u*)(w * 1056 + l16) = ln_mod(ln_norm(v), tt, sh);
    BAR();
    bst(ws, (l >> 4) * 1024 + (l & 15) * 16, oH, ldsr((l & 3) * 1056 + w * 256 + (l >> 2) * 16));
    SST(5, sg);
    signal(myfl, ++cnt);
  }
}

DEV void stage_g(const Params& p, const Dn& d, int q, int w, int l, int& budget) {
  const Stg s = stg(l);
  const char* sg0 = (const char*)p.wg + q * 128 + (w >> 1) * 64;
  const char* su0 = (const char*)p.wu + q * 128 + (w >> 1) * 64;
  v4u W[64];
  load_w<16>(W, w, l, ((s.tile >> 1) * 32 + 8 * s.g0 + s.q) * 3584 + (s.tile & 1) * 32 + s.half * 16,
             [&](int c, int i) { return ((c & 1) ? su0 : sg0) + ((c >> 1) * 64 + 16 * (i & 1) + 4 * (i >> 1)) * 3584; });
  v4u V[64];
#pragma unroll
  for (int i = 0; i < 64; ++i) V[i] = W[((i >> 3) * 2 + ((i >> 1) & 1)) * 4 + ((i >> 2) & 1) * 2 + (i & 1)];
  const int pi = 128 + q; PST(2);
  const int ks = q * 2 + (w >> 1);
  const unsigned ao = (w & 1) * 131072 + (ks / 14) * 32768 + hm((ks % 14) * 1024);
  stream<16, 0, true>(V, (((l & 15) >> 2) * 16 + (l >> 4) * 4 + (l & 3)) * 16, d.fl + FL_R + (l & 15) * 128, 1, (l >> 3) & 1, d.fl + FL_G + q * 128, budget,
    [&](int sg) { return d.at(RG_H2 + (sg >> 5), (sg & 31) * 65536 + (w & 1) * 32768); }, [&](int sg) {},
    [&](v4f (&a)[4], int sg) {
      v4u y;
#pragma unroll
      for (int i = 0; i < 4; ++i) {
        const unsigned gg = pk2(a[i >> 1][2 * (i & 1)], a[i >> 1][2 * (i & 1) + 1]), uu = pk2(a[2 + (i >> 1)][2 * (i & 1)], a[2 + (i >> 1)][2 * (i & 1) + 1]);
        y[i] = pk2(blo(gg) * blo(uu), bhi(gg) * bhi(uu));
      }
      st_s<0>(l * 16, d.at(RG_ACT + (sg >> 3), (sg & 7) * 262144 + ao), y);
      SST(7, sg);
    },
    [&](v4f (&a)[4], int sg) {});
}

DEV void stage_d(const Params& p, const Dn& d, int q, int w, int l, int& budget) {
  const Stg s = stg(l);
  const int T = q & 1, cb = q >> 1;
  const char* sd = (const char*)p.wd + w * 448 * 1024 + cb * 128;
  v4u W[56];
  load_w<14>(W, w, l, (4 * s.g0 + s.q) * 1024 + s.tile * 32 + s.half * 16, [&](int c, int i) { return sd + (c * 32 + 8 * (i & 1) + 16 * (i >> 1)) * 1024; });
  const int pi = 160 + q; PST(2);
  const int tok = (w & 1) * 8 + (l >> 3), col = cb * 64 + (l & 7) * 8;
  v4u gb = *(const v4u*)(p.ws + WS_CTL + CTL_MOD + 5120 + col * 2);
  OPQ_V(gb);
  stream<14, 1, false>(W, l * 16, d.fl + FL_G + (l < 28 ? l : 0) * 128, 0, 0, nullptr, budget,
    [&](int sg) { return d.at(RG_ACT + (sg >> 3), (sg & 7) * 262144 + T * 131072 + w * 32768); },
    [&](int sg) { ldx<14, 0>(hm(tok * 1024) + col * 2, d.at(RG_X2 + (sg >> 5), (sg & 31) * 65536 + T * 32768)); },
    [&](v4f (&a)[4], int sg) {
#pragma unroll
      for (int t = 0; t < 4; ++t) *(LAS v4f*)((sg & 1) * 17408 + w * 4352 + (l & 15) * 272 + t * 64 + (l >> 4) * 16) = a[t];
    },
    [&](v4f (&a)[4], int sg) {
      if (w < 2) {
        v2u xl, xh;
        asm volatile("v_mov_b64 %0, v[248:249]\n\tv_mov_b64 %1, v[250:251]" : "=&v"(xl), "=&v"(xh));
        const v4u x2 = {xl[0], xl[1], xh[0], xh[1]};
        const unsigned ra = (sg & 1) * 17408 + tok * 272 + (l & 7) * 32;
        const v4f s0 = (ldsf(ra) + ldsf(ra + 4352)) + (ldsf(ra + 8704) + ldsf(ra + 13056)), s1 = (ldsf(ra + 16) + ldsf(ra + 4368)) + (ldsf(ra + 8720) + ldsf(ra + 13072));
        v4u o;
#pragma unroll
        for (int j = 0; j < 4; ++j) {
          const unsigned f = j < 2 ? pk2(s0[2 * j], s0[2 * j + 1]) : pk2(s1[2 * j - 4], s1[2 * j - 3]);
          const unsigned gq = pk2(blo(gb[j]) * blo(f), bhi(gb[j]) * bhi(f));
          o[j] = pk2(blo(x2[j]) + blo(gq), bhi(x2[j]) + bhi(gq));
        }
        if (CFG_OWT) st_sc1(p.out + (sg * 32 + T * 16 + tok) * 512 + col, o);
        else *(v4u*)(p.out + (sg * 32 + T * 16 + tok) * 512 + col) = o;
        SST(9, sg);
      }
    });
}

DEV void kv_prefetch(const Params& p, int q, int w, int l, int& budget) {
  const unsigned* par = (const unsigned*)(p.ws + WS_CTL + CTL_PAR);
  const rsrc_t ws = __builtin_amdgcn_make_buffer_rsrc(p.ws, 0, 0x7fffffff, 0x00020000);
  const unsigned oKV = (RG_KV + q) * REG + PARW(RG_KV + q) + hm(w * 4096);
  const char* pA = p.ws + WS_COMM + (comm_vote(p.ws) & 1) * 8192 + COMM_PROGA + q * 128;
  for (int st = 1; st + CFG_PLEAD < 64 && CFG_PF; ++st) {
    wait_ge<CFG_PPOLL>(pA, st, budget);
    v4u t[4];
#pragma unroll
    for (int k = 0; k < 4; ++k) t[k] = bld(ws, l * 16 + k * 1024, oKV + (st + CFG_PLEAD) * 32768);
    asm volatile("" ::"v"(t[0]), "v"(t[1]), "v"(t[2]), "v"(t[3]));
  }
}

DEV void down_phase(const Params& p, int pi, int w, int l, int& budget) {
  Dn d; d.ws = p.ws; d.par = (const unsigned*)(p.ws + WS_CTL + CTL_PAR);
  d.mask = __ballot(d.par[RG_O + (l < NPROBE - RG_O ? l : 0)] != 0);
  asm volatile("s_nop 4" : "+s"(d.mask));
  d.fl = (char*)d.at(RG_FL, 0);
  const int q = pi & 31;
  if (pi >= 128) for (int z = 0; z < (pi < 160 ? CFG_GDEL : 2 * CFG_GDEL); ++z) __builtin_amdgcn_s_sleep(32);
  if (pi < 96) stage_t(p, d, q, w, l, budget);
  else if (pi < 128) stage_w(p, d, q, w, l, budget);
  else if (pi < 160) { if (q < 28) stage_g(p, d, q, w, l, budget); }
  else if (q < 16) stage_d(p, d, q, w, l, budget);
  else stage_r(p, d, q - 16, w, l, budget);
}

__global__ void __launch_bounds__(256, 1) k_main(Params p) {
  extern __shared__ __attribute__((aligned(16))) unsigned lds[];
  if (CFG_CODEKB && threadIdx.x >= 64) {
    const char* pc;
    asm volatile("s_getpc_b64 %0" : "=s"(pc));
    const unsigned o = ((blockIdx.x >> 3) * 192 + threadIdx.x - 64) * 16;
    if (o < CFG_CODEKB * 1024) { const v4u t = *(const GAS v4u*)(((size_t)pc & ~(size_t)255) + o); asm volatile("" ::"v"(t)); }
  }
  if (threadIdx.x == 0) {
    unsigned x = xcc_id() & 7, t = 0;
    for (int tries = 0; tries < 8; ++tries) {
      t = __hip_atomic_fetch_add((GAS unsigned*)(p.ws + WS_CTL + CTL_TICKET + x * 128), 1u, __ATOMIC_RELAXED, __HIP_MEMORY_SCOPE_AGENT);
      if (t < 32) break;
      x = (x + 1) & 7;
    }
    lds[0] = x * 32 + (t & 31);
  }
  __syncthreads();
  const int role = __builtin_amdgcn_readfirstlane(lds[0]);
  __syncthreads();
  const int x = role >> 5, loc = role & 31;
  if (x < 4 && loc < 16) {
    a_role(p, x * 8 + (loc >> 1), loc & 1, x);
    return;
  }
  const int pi = x < 4 ? (loc < 24 ? 32 + x * 8 + loc - 16 : x * 8 + loc - 24) : role - 64;
  const int w = __builtin_amdgcn_readfirstlane(threadIdx.x >> 6), l = threadIdx.x & 63;
  int budget = 1 << 18;
  if (pi < 64 && CFG_MW) {
    const int k = loc - 16, tid = threadIdx.x;
    const size_t wo = (size_t)(k * 32 + (tid >> 7)) * 8192 + x * 2048 + (tid & 127) * 16;
    warm((const char*)p.Wk + wo, 16384); warm((const char*)p.Wv + wo, 16384); warm((const char*)p.Wq + wo, 16384);
    warm((const char*)p.Wo + (size_t)x * 1048576 + k * 65536 + tid * 16, 4096);
  }
  if (pi >= 32) {
    qkv_phase(p, pi, pi >> 6, pi < 64 ? (pi - 32) >> 1 : (pi >> 2) & 15, pi < 64 ? pi & 1 : pi & 3, w, l, budget);
    PST(1);
    if (pi >= 64) { down_phase(p, pi, w, l, budget); SDUMP(); }
  } else kv_prefetch(p, pi, w, l, budget);
  PST(7);
}

extern "C" void kernel_launch(void* const* d_in, const int* in_sizes, int n_in, void* d_out, int out_size, void* d_ws, size_t ws_size, hipStream_t stream) {
  Params p;
  p.x = (const u16*)d_in[0]; p.mem0 = (const u16*)d_in[1]; p.Wq = (const u16*)d_in[2]; p.Wk = (const u16*)d_in[3]; p.Wv = (const u16*)d_in[4]; p.Wo = (const u16*)d_in[5];
  p.wg = (const u16*)d_in[8]; p.wu = (const u16*)d_in[9]; p.wd = (const u16*)d_in[10]; p.c = (const u16*)d_in[11]; p.wm = (const u16*)d_in[12];
  p.out = (u16*)d_out; p.d1 = g_d1; p.d2 = g_d2;
  p.ws = (char*)(((size_t)d_ws + (2u << 20) - 1) & ~(size_t)((2u << 20) - 1));
  (void)hipFuncSetAttribute((const void*)k_main, hipFuncAttributeMaxDynamicSharedMemorySize, 163840);
  k_prep<<<PREP_W0 + CFG_PW * 256, 256, 0, stream>>>(p);
  k_main<<<256, 256, 163840, stream>>>(p);
}
```
